```python
import torch

def _softmax(x, axis=-1):
    return torch.softmax(x, dim=axis)
def _gelu(x):
    return torch.nn.functional.gelu(x, approximate="tanh")
def _silu(x):
    return torch.nn.functional.silu(x)
def _sigmoid(x):
    return torch.sigmoid(x)
def _layernorm(x, axis=-1, eps=1e-5):
    m = x.mean(dim=axis, keepdim=True)
    v = x.var(dim=axis, keepdim=True, unbiased=False)
    return (x - m) / torch.sqrt(v + eps)
def _scatter_add(tgt, idx, src, axis=0):
    return tgt.clone().index_add_(axis, idx, src)
def _segment_sum(src, seg, n):
    out = torch.zeros((n,) + src.shape[1:], dtype=src.dtype)
    return out.index_add_(0, seg, src)
def _pad(x, pw):
    flat = tuple(v for pair in reversed(pw) for v in pair)
    return torch.nn.functional.pad(x, flat)

kernel_name = 'standalone__gqa_block_cached__sampling'

def setup_inputs():

    _w = (lambda a: (lambda t: t.to(torch.bfloat16) if t.is_floating_point() else t)(torch.as_tensor(a)))
    B, Sq, D = 32, 4, 1024
    Hq, Hkv, Sk, Dh = 32, 4, 2048, 128
    assert Hq % Hkv == 0, (Hq, Hkv)
    return {
        "x": _w((lambda *s: torch.randn(*s, dtype=torch.bfloat16) * 0.8)(B, Sq, D)),
        "k_cache": _w((lambda *s: torch.randn(*s, dtype=torch.bfloat16) * 0.8)(B, Hkv, Sk, Dh)),
        "v_cache": _w((lambda *s: torch.randn(*s, dtype=torch.bfloat16) * 0.05)(B, Hkv, Sk, Dh)),
        "wq": _w((lambda *s: torch.randn(*s, dtype=torch.bfloat16) * 0.8)(D, Hq * Dh) * (D ** -0.5)),
        "wo": _w((lambda *s: torch.randn(*s, dtype=torch.bfloat16) * 0.05)(Hq * Dh, D) * ((Hq * Dh) ** -0.5)),
    }

def reference(x, k_cache, v_cache, wq, wo):
    B, Sq, D = x.shape
    Hq, Dh = 32, 128
    Hkv = k_cache.shape[1]
    rep = Hq // Hkv
    q = (lambda x, *d: x.permute(*d))((x @ wq).reshape(B, Sq, Hq, Dh), 0, 2, 1, 3)
    k = (lambda x, n, axis: torch.repeat_interleave(x, n, dim=axis))(k_cache, rep, axis=1)
    v = (lambda x, n, axis: torch.repeat_interleave(x, n, dim=axis))(v_cache, rep, axis=1)
    scores = torch.einsum("bhqd,bhkd->bhqk", q, k) * (Dh ** -0.5)
    p = _softmax(scores, axis=-1)
    o = torch.einsum("bhqk,bhkd->bhqd", p, v)
    return (lambda x, *d: x.permute(*d))(o, 0, 2, 1, 3).reshape(B, Sq, Hq * Dh) @ wo
```

```cpp
#include <hip/hip_runtime.h>
#include <hip/hip_bf16.h>
#include <stdint.h>

typedef unsigned u32x4 __attribute__((ext_vector_type(4)));
typedef unsigned u32x2 __attribute__((ext_vector_type(2)));
typedef float f32x4 __attribute__((ext_vector_type(4)));
typedef short bf16x8 __attribute__((ext_vector_type(8)));
typedef unsigned short u16;

constexpr int B = 32, SQ = 4, DM = 1024, HQ = 32, HKV = 4, SK = 2048, DH = 128;
constexpr int NROW = B * SQ;
constexpr int NQ = HQ * DH;

__device__ __forceinline__ f32x4 mfma16(u32x4 a, u32x4 b, f32x4 c) {
  return __builtin_amdgcn_mfma_f32_16x16x32_bf16(__builtin_bit_cast(bf16x8, a), __builtin_bit_cast(bf16x8, b), c, 0, 0, 0);
}
__device__ __forceinline__ unsigned cvtpk(float lo, float hi) {
  unsigned r; asm("v_cvt_pk_bf16_f32 %0, %1, %2" : "=v"(r) : "v"(lo), "v"(hi)); return r;
}
__device__ __forceinline__ unsigned perm_lo(unsigned a, unsigned b) { return __builtin_amdgcn_perm(b, a, 0x05040100u); }
__device__ __forceinline__ unsigned perm_hi(unsigned a, unsigned b) { return __builtin_amdgcn_perm(b, a, 0x07060302u); }

template <int KD, int N, class Epi>
__device__ __forceinline__ void proj_tile(const u16* __restrict__ X, const u16* __restrict__ W, int row0, int col0, float* red, Epi epi) {
  const int tid = threadIdx.x, lane = tid & 63, w = tid >> 6, c = lane & 15, g = lane >> 4;
  constexpr int KW = KD / 8;
  f32x4 acc[2][4];
#pragma unroll
  for (int t = 0; t < 2; ++t)
#pragma unroll
    for (int j = 0; j < 4; ++j) acc[t][j] = f32x4{0.f, 0.f, 0.f, 0.f};
  for (int k0 = w * KW; k0 < (w + 1) * KW; k0 += 128) {
    u32x2 wv[4][8];
    u32x4 xv[4][2];
#pragma unroll
    for (int s = 0; s < 4; ++s) {
#pragma unroll
      for (int i = 0; i < 8; ++i) wv[s][i] = *(const u32x2*)(W + (size_t)(k0 + 32 * s + 8 * g + i) * N + col0 + 4 * c);
#pragma unroll
      for (int t = 0; t < 2; ++t) xv[s][t] = *(const u32x4*)(X + (size_t)(row0 + 16 * t + c) * KD + k0 + 32 * s + 8 * g);
    }
#pragma unroll
    for (int s = 0; s < 4; ++s)
#pragma unroll
      for (int j = 0; j < 4; ++j) {
        u32x4 a;
#pragma unroll
        for (int u = 0; u < 4; ++u) a[u] = (j & 1) ? perm_hi(wv[s][2 * u][j >> 1], wv[s][2 * u + 1][j >> 1]) : perm_lo(wv[s][2 * u][j >> 1], wv[s][2 * u + 1][j >> 1]);
#pragma unroll
        for (int t = 0; t < 2; ++t) acc[t][j] = mfma16(a, xv[s][t], acc[t][j]);
      }
  }
  f32x4* r4 = (f32x4*)red;
#pragma unroll
  for (int t = 0; t < 2; ++t)
#pragma unroll
    for (int j = 0; j < 4; ++j) r4[(w * 8 + t * 4 + j) * 64 + lane] = acc[t][j];
  __syncthreads();
  if (tid < 256) {
    const int t = tid >> 7, half = (tid >> 6) & 1;
    float v[8];
#pragma unroll
    for (int j = 0; j < 4; ++j) {
      f32x4 s = {0.f, 0.f, 0.f, 0.f};
#pragma unroll
      for (int ww = 0; ww < 8; ++ww) s += r4[(ww * 8 + t * 4 + j) * 64 + lane];
      v[j] = half ? s[2] : s[0];
      v[4 + j] = half ? s[3] : s[1];
    }
    u32x4 o = {cvtpk(v[0], v[1]), cvtpk(v[2], v[3]), cvtpk(v[4], v[5]), cvtpk(v[6], v[7])};
    epi(row0 + 16 * t + c, col0 + 16 * g + 8 * half, o);
  }
}

__global__ __launch_bounds__(512) void k_qproj(const u16* __restrict__ x, const u16* __restrict__ wq, u16* __restrict__ qf) {
  __shared__ __attribute__((aligned(16))) float red[8 * 8 * 64 * 4];
  const int bid = blockIdx.x, xcd = bid & 7, i = bid >> 3;
  const int colt = xcd * 8 + (i >> 2), rowt = i & 3;
  proj_tile<DM, NQ>(x, wq, rowt * 32, colt * 64, red, [&](int row, int col, u32x4 o) {
    const int b = row >> 2, sq = row & 3, h = col >> 7, d = col & 127, hkv = h >> 3, r = h & 7;
    const int q = r * 4 + sq, qt = q >> 4, ch = d >> 3, s = ch >> 2, gg = ch & 3;
    *(u32x4*)(qf + ((((size_t)(b * HKV + hkv) * 2 + qt) * 4 + s) * 64 + gg * 16 + (q & 15)) * 8) = o;
  });
}

__global__ __launch_bounds__(512) void k_oproj(const u16* __restrict__ o, const u16* __restrict__ wo, u16* __restrict__ out) {
  __shared__ __attribute__((aligned(16))) float red[8 * 8 * 64 * 4];
  const int bid = blockIdx.x;
  const int colt = bid >> 2, rowt = bid & 3;
  proj_tile<NQ, DM>(o, wo, rowt * 32, colt * 64, red, [&](int row, int col, u32x4 v) { *(u32x4*)(out + (size_t)row * DM + col) = v; });
}

__device__ __forceinline__ float xg_max(float x) {
  auto s = __builtin_amdgcn_permlane16_swap(__float_as_uint(x), __float_as_uint(x), false, false);
  x = fmaxf(__uint_as_float(s[0]), __uint_as_float(s[1]));
  auto t = __builtin_amdgcn_permlane32_swap(__float_as_uint(x), __float_as_uint(x), false, false);
  return fmaxf(__uint_as_float(t[0]), __uint_as_float(t[1]));
}
__device__ __forceinline__ float xg_sum(float x) {
  auto s = __builtin_amdgcn_permlane16_swap(__float_as_uint(x), __float_as_uint(x), false, false);
  x = __uint_as_float(s[0]) + __uint_as_float(s[1]);
  auto t = __builtin_amdgcn_permlane32_swap(__float_as_uint(x), __float_as_uint(x), false, false);
  return __uint_as_float(t[0]) + __uint_as_float(t[1]);
}

#define LDNT(p) (*(const u32x4*)(p))
#define SB() __builtin_amdgcn_sched_barrier(0)

__global__ __launch_bounds__(512, 2) void k_attn(const u16* __restrict__ qf, const u16* __restrict__ kc, const u16* __restrict__ vc,
                                                float* __restrict__ po, float* __restrict__ pml) {
  extern __shared__ __attribute__((aligned(16))) float lds[];
  const int tid = threadIdx.x, lane = tid & 63, w = __builtin_amdgcn_readfirstlane(tid >> 6), c = lane & 15, g = lane >> 4;
  const int wg = blockIdx.x, pair = wg >> 1, half = wg & 1;
  const size_t base = ((size_t)pair * SK + half * 1024 + w * 128) * DH;
  const u16* kp = kc + base + (size_t)(4 * (c & 3) + (c >> 2)) * DH + g * 8;
  const u16* vp = vc + base + (size_t)g * DH + c * 8;
  u32x4 q[2][4];
#pragma unroll
  for (int t = 0; t < 2; ++t)
#pragma unroll
    for (int s = 0; s < 4; ++s) q[t][s] = *(const u32x4*)(qf + (((size_t)pair * 2 + t) * 4 + s) * 512 + lane * 8);
  u32x4 kb[4][8], vb[4][8];
#pragma unroll
  for (int blk = 0; blk < 4; ++blk)
#pragma unroll
    for (int i = 0; i < 8; ++i) kb[blk][i] = LDNT(kp + (size_t)(blk * 32 + (i >> 2) * 16) * DH + (i & 3) * 32);
  SB();
  f32x4 sc[4][2][2];
#pragma unroll
  for (int blk = 0; blk < 4; ++blk) {
#pragma unroll
    for (int t = 0; t < 2; ++t)
#pragma unroll
      for (int qt = 0; qt < 2; ++qt) {
        f32x4 a = {0.f, 0.f, 0.f, 0.f};
#pragma unroll
        for (int s = 0; s < 4; ++s) a = mfma16(kb[blk][t * 4 + s], q[qt][s], a);
        sc[blk][t][qt] = a;
      }
    SB();
#pragma unroll
    for (int i = 0; i < 8; ++i) vb[blk][i] = LDNT(vp + (size_t)(blk * 32 + i * 4) * DH);
    SB();
  }
  constexpr float C2 = 0.088388347648318440f * 1.4426950408889634f;
  float m[2], l[2];
  u32x4 p[4][2];
#pragma unroll
  for (int qt = 0; qt < 2; ++qt) {
    float mx = sc[0][0][qt][0];
#pragma unroll
    for (int blk = 0; blk < 4; ++blk)
#pragma unroll
      for (int t = 0; t < 2; ++t)
#pragma unroll
        for (int r = 0; r < 4; ++r) mx = fmaxf(mx, sc[blk][t][qt][r]);
    mx = xg_max(mx) * C2;
    float sum = 0.f;
#pragma unroll
    for (int blk = 0; blk < 4; ++blk) {
      float e[8];
#pragma unroll
      for (int j = 0; j < 8; ++j) { e[j] = __builtin_amdgcn_exp2f(fmaf(sc[blk][j >> 2][qt][j & 3], C2, -mx)); sum += e[j]; }
      p[blk][qt] = u32x4{cvtpk(e[0], e[1]), cvtpk(e[2], e[3]), cvtpk(e[4], e[5]), cvtpk(e[6], e[7])};
    }
    m[qt] = mx; l[qt] = xg_sum(sum);
  }
  SB();
  f32x4 o[8][2];
#pragma unroll
  for (int j = 0; j < 8; ++j) { o[j][0] = f32x4{0.f, 0.f, 0.f, 0.f}; o[j][1] = f32x4{0.f, 0.f, 0.f, 0.f}; }
#pragma unroll
  for (int blk = 0; blk < 4; ++blk) {
#pragma unroll
    for (int j = 0; j < 8; ++j) {
      u32x4 a;
#pragma unroll
      for (int u = 0; u < 4; ++u) a[u] = (j & 1) ? perm_hi(vb[blk][2 * u][j >> 1], vb[blk][2 * u + 1][j >> 1]) : perm_lo(vb[blk][2 * u][j >> 1], vb[blk][2 * u + 1][j >> 1]);
      o[j][0] = mfma16(a, p[blk][0], o[j][0]);
      o[j][1] = mfma16(a, p[blk][1], o[j][1]);
    }
    SB();
  }
  float* ml = lds;
  f32x4* red = (f32x4*)(lds + 512);
  if (g == 0) {
#pragma unroll
    for (int qt = 0; qt < 2; ++qt) { ml[(w * 32 + qt * 16 + c) * 2] = m[qt]; ml[(w * 32 + qt * 16 + c) * 2 + 1] = l[qt]; }
  }
  __syncthreads();
#pragma unroll
  for (int qt = 0; qt < 2; ++qt) {
    float M = ml[(qt * 16 + c) * 2];
#pragma unroll
    for (int ww = 1; ww < 8; ++ww) M = fmaxf(M, ml[(ww * 32 + qt * 16 + c) * 2]);
    const float f = __builtin_amdgcn_exp2f(m[qt] - M);
#pragma unroll
    for (int j = 0; j < 8; ++j) red[(w * 16 + j * 2 + qt) * 64 + lane] = o[j][qt] * f;
    if (w == 0 && g == 0) {
      float L = 0.f;
#pragma unroll
      for (int ww = 0; ww < 8; ++ww) L += ml[(ww * 32 + qt * 16 + c) * 2 + 1] * __builtin_amdgcn_exp2f(ml[(ww * 32 + qt * 16 + c) * 2] - M);
      pml[((size_t)wg * 32 + qt * 16 + c) * 2] = M;
      pml[((size_t)wg * 32 + qt * 16 + c) * 2 + 1] = L;
    }
  }
  __syncthreads();
#pragma unroll
  for (int qt = 0; qt < 2; ++qt) {
    f32x4 s = {0.f, 0.f, 0.f, 0.f};
#pragma unroll
    for (int ww = 0; ww < 8; ++ww) s += red[(ww * 16 + w * 2 + qt) * 64 + lane];
#pragma unroll
    for (int r = 0; r < 4; ++r) po[((size_t)wg * 32 + qt * 16 + c) * 128 + 8 * (4 * g + r) + w] = s[r];
  }
}

__global__ __launch_bounds__(256) void k_comb(const float* __restrict__ po, const float* __restrict__ pml, u16* __restrict__ o) {
  const int idx = blockIdx.x * 256 + threadIdx.x;
  const int pair = idx >> 11, q = (idx >> 6) & 31, d = (idx & 63) * 2;
  const float m0 = pml[((pair * 2) * 32 + q) * 2], l0 = pml[((pair * 2) * 32 + q) * 2 + 1];
  const float m1 = pml[((pair * 2 + 1) * 32 + q) * 2], l1 = pml[((pair * 2 + 1) * 32 + q) * 2 + 1];
  const float M = fmaxf(m0, m1), f0 = __builtin_amdgcn_exp2f(m0 - M), f1 = __builtin_amdgcn_exp2f(m1 - M);
  const float inv = 1.f / (f0 * l0 + f1 * l1);
  const float2 a = *(const float2*)(po + ((size_t)(pair * 2) * 32 + q) * 128 + d), b2 = *(const float2*)(po + ((size_t)(pair * 2 + 1) * 32 + q) * 128 + d);
  const int b = pair >> 2, hkv = pair & 3, r = q >> 2, sq = q & 3;
  *(unsigned*)(o + (size_t)(b * 4 + sq) * NQ + (hkv * 8 + r) * 128 + d) = cvtpk((a.x * f0 + b2.x * f1) * inv, (a.y * f0 + b2.y * f1) * inv);
}

extern "C" void kernel_launch(void* const* d_in, const int* in_sizes, int n_in, void* d_out, int out_size, void* d_ws, size_t ws_size,
                              hipStream_t stream) {
  const u16* x = (const u16*)d_in[0];
  const u16* kc = (const u16*)d_in[1];
  const u16* vc = (const u16*)d_in[2];
  const u16* wq = (const u16*)d_in[3];
  const u16* wo = (const u16*)d_in[4];
  char* ws = (char*)d_ws;
  u16* qf = (u16*)ws;
  u16* ob = (u16*)(ws + (1 << 20));
  float* pml = (float*)(ws + (2 << 20));
  float* po = (float*)(ws + (3 << 20));
  constexpr int LDS_ATTN = 2048 + 8 * 16 * 64 * 16;
  hipFuncSetAttribute((const void*)k_attn, hipFuncAttributeMaxDynamicSharedMemorySize, LDS_ATTN);
  k_qproj<<<256, 512, 0, stream>>>(x, wq, qf);
  k_attn<<<256, 512, LDS_ATTN, stream>>>(qf, kc, vc, po, pml);
  k_comb<<<1024, 256, 0, stream>>>(po, pml, ob);
  k_oproj<<<64, 512, 0, stream>>>(ob, wo, (u16*)d_out);
}
```

```cpp
#include <hip/hip_runtime.h>
#include <stdint.h>

typedef unsigned u32x2 __attribute__((ext_vector_type(2)));
typedef unsigned u32x4 __attribute__((ext_vector_type(4)));
typedef unsigned u32x16 __attribute__((ext_vector_type(16)));
typedef unsigned u32x32 __attribute__((ext_vector_type(32)));
typedef int i32x4 __attribute__((ext_vector_type(4)));
typedef float f32x2 __attribute__((ext_vector_type(2)));
typedef float f32x4 __attribute__((ext_vector_type(4)));
typedef float f32x32 __attribute__((ext_vector_type(32)));
typedef short bf16x8 __attribute__((ext_vector_type(8)));
typedef short s16x4 __attribute__((ext_vector_type(4)));
typedef unsigned long long u64;
#define LAS __attribute__((address_space(3)))
#define SB() __builtin_amdgcn_sched_barrier(0)
#define MEMBAR() asm volatile("" ::: "memory")

constexpr int WIN1 = 8, WIN2 = 16;
constexpr int WINP = 6;
constexpr int QT0 = 150, QT1 = 16;
constexpr int OT0 = 640, OT1 = 16;

constexpr int WS_E = 0, WS_ML = 8192, WS_FLAG = 192 << 10, WS_HINT = 256 << 10, WS_PH = 320 << 10, WS_Q = 1 << 20, WS_O = 4 << 20, WS_P = 8 << 20,
              WS_BYTES = 10 << 20;
constexpr int L_SCR = 131072, L_ML = L_SCR + 24576, L_F = L_ML + 1024, L_CNT = L_F + 512, L_SINK = L_CNT + 128, LDS_BYTES = L_SINK + 1024;
enum { C_QRED = 0, C_QST, C_FLAG, C_QRDY, C_STEP, C_ML, C_MLRD, C_F, C_LAST, C_RDY, C_TAG };
constexpr int SC1 = 16;
constexpr int FAR = 0x7F000000;
constexpr unsigned SPIN_CAP = 1u << 16;
constexpr float C2 = 0.088388347648318440f * 1.4426950408889634f;

template <int N> using ic = std::integral_constant<int, N>;
template <int I, int N, class F> __device__ __forceinline__ void sfor(F&& f) {
  if constexpr (I < N) { f(ic<I>{}); sfor<I + 1, N>(f); }
}
__device__ __forceinline__ f32x4 mfma16(u32x4 a, u32x4 b, f32x4 c) {
  return __builtin_amdgcn_mfma_f32_16x16x32_bf16(__builtin_bit_cast(bf16x8, a), __builtin_bit_cast(bf16x8, b), c, 0, 0, 0);
}
__device__ __forceinline__ unsigned cvtpk(float lo, float hi) {
  typedef __bf16 b2 __attribute__((ext_vector_type(2)));
  return __builtin_bit_cast(unsigned, __builtin_convertvector(f32x2{lo, hi}, b2));
}
__device__ __forceinline__ float xg_max(float x) {
  auto s = __builtin_amdgcn_permlane16_swap(__float_as_uint(x), __float_as_uint(x), false, false);
  x = fmaxf(__uint_as_float(s[0]), __uint_as_float(s[1]));
  auto t = __builtin_amdgcn_permlane32_swap(__float_as_uint(x), __float_as_uint(x), false, false);
  return fmaxf(__uint_as_float(t[0]), __uint_as_float(t[1]));
}
__device__ __forceinline__ float xg_sum(float x) {
  auto s = __builtin_amdgcn_permlane16_swap(__float_as_uint(x), __float_as_uint(x), false, false);
  x = __uint_as_float(s[0]) + __uint_as_float(s[1]);
  auto t = __builtin_amdgcn_permlane32_swap(__float_as_uint(x), __float_as_uint(x), false, false);
  return __uint_as_float(t[0]) + __uint_as_float(t[1]);
}
__device__ __forceinline__ unsigned tag8(float x, unsigned tag) { return (__float_as_uint(x) & ~0xFFu) | (tag & 0xFFu); }
__device__ __forceinline__ unsigned nomark(unsigned x) { return min(x, 0xFFFFFFFEu); }

template <int WHICH> __device__ __forceinline__ void lds_signal() {
  asm volatile("s_mov_b64 exec, 1\n\ts_waitcnt lgkmcnt(0)\n\tds_add_u32 %0, %1 offset:%2\n\ts_mov_b64 exec, -1"
               :: "v"((unsigned)L_CNT), "v"(1u), "n"(4 * WHICH) : "memory");
}
template <int WHICH> __device__ __forceinline__ void lds_bump() {
  asm volatile("s_mov_b64 exec, 1\n\tds_add_u32 %0, %1 offset:%2\n\ts_mov_b64 exec, -1" :: "v"((unsigned)L_CNT), "v"(1u), "n"(4 * WHICH) : "memory");
}
template <int WHICH, unsigned N> __device__ __forceinline__ void lds_wait() {
  MEMBAR();
  while ((unsigned)__builtin_amdgcn_readfirstlane(*(volatile LAS unsigned*)(L_CNT + 4 * WHICH)) < N) __builtin_amdgcn_s_sleep(1);
  MEMBAR();
}
template <int WHICH, int N> __device__ __forceinline__ void lds_spin() {
  unsigned t;
  asm volatile("1:\n\tv_mov_b32 %0, %1\n\tds_read_b32 %0, %0\n\ts_waitcnt lgkmcnt(0)\n\tv_cmp_gt_u32 vcc, %2, %0\n\ts_cbranch_vccnz 1b"
               : "=&v"(t) : "n"(L_CNT + 4 * WHICH), "n"(N) : "vcc", "memory");
}
__device__ __forceinline__ unsigned now() { return (unsigned)__builtin_amdgcn_s_memrealtime(); }
__device__ __forceinline__ void until(unsigned t) {
  while ((int)(now() - t) < 0) __builtin_amdgcn_s_sleep(2);
  MEMBAR();
}

__device__ __forceinline__ i32x4 mkrs(const void* p, int bytes) {
  const u64 a = (u64)p;
  return i32x4{(int)(unsigned)a, (int)((a >> 32) & 0xffff), bytes, 0x00020000};
}
template <int N> __device__ __forceinline__ void wait_vm() { asm volatile("s_waitcnt vmcnt(%0)" ::"n"(N) : "memory"); }
template <int W>
__device__ __forceinline__ void dma_tile(i32x4 rs, unsigned ldsb, int soff, const int (&v)[4]) {
  asm volatile("s_mov_b32 m0, %1\n\ts_waitcnt vmcnt(%7) lgkmcnt(0)\n\t"
               "buffer_load_dwordx4 %3, %0, %2 offen nt lds\n\ts_waitcnt vmcnt(%7)\n\t"
               "buffer_load_dwordx4 %4, %0, %2 offen offset:1024 nt lds\n\ts_waitcnt vmcnt(%7)\n\t"
               "buffer_load_dwordx4 %5, %0, %2 offen offset:2048 nt lds\n\ts_waitcnt vmcnt(%7)\n\t"
               "buffer_load_dwordx4 %6, %0, %2 offen offset:3072 nt lds"
               :: "s"(rs), "s"(ldsb), "s"(soff), "v"(v[0]), "v"(v[1]), "v"(v[2]), "v"(v[3]), "n"(W - 1) : "memory");
}
__device__ __forceinline__ void touch_lines(i32x4 rs, int voff, int soff) {
  asm volatile("s_mov_b32 m0, %3\n\ts_nop 0\n\tbuffer_load_dword %0, %1, %2 offen lds" :: "v"(voff), "s"(rs), "s"(soff), "n"(L_SINK) : "memory");
}
__device__ __forceinline__ void st_dev(__amdgpu_buffer_rsrc_t rs, int off, u32x4 d) { __builtin_amdgcn_raw_buffer_store_b128(d, rs, off, 0, SC1); }

template <int N>
__device__ __forceinline__ void await_hint(const char* p, unsigned tag) {
  _Pragma("nounroll") for (int spin = 0; spin < 64; ++spin) {
    unsigned bad = 0;
    if constexpr (N == 16) {
      u32x16 y;
      asm volatile("s_load_dwordx16 %0, %1, 0x0 glc\n\ts_waitcnt lgkmcnt(0)" : "=s"(y) : "s"(p) : "memory");
#pragma unroll
      for (int i = 0; i < 16; ++i) bad |= y[i] ^ tag;
    } else {
      u32x4 y;
      asm volatile("s_load_dwordx4 %0, %1, 0x0 glc\n\ts_waitcnt lgkmcnt(0)" : "=s"(y) : "s"(p) : "memory");
#pragma unroll
      for (int i = 0; i < 4; ++i) bad |= y[i] ^ tag;
    }
    if (!bad) break;
  }
}

__device__ __forceinline__ void wtrans(const u32x4 (&w)[4], u32x4 (&a)[4]) {
#pragma unroll
  for (int i = 0; i < 4; ++i)
#pragma unroll
    for (int p = 0; p < 2; ++p) {
      const unsigned e0 = __builtin_amdgcn_update_dpp(w[i][p], w[i][2 + p], 0x128, 0xf, 0xc, false);
      const unsigned e1 = __builtin_amdgcn_update_dpp(w[i][2 + p], w[i][p], 0x128, 0xf, 0x3, false);
      a[2 * p][i] = __builtin_amdgcn_perm(e1, e0, 0x05040100u);
      a[2 * p + 1][i] = __builtin_amdgcn_perm(e1, e0, 0x07060302u);
    }
}
template <class F>
__device__ __forceinline__ void proj_reduce(LAS char* const (&ra)[4], const f32x4 (&acc)[2][4], float (&r)[8], F&& meet) {
#pragma unroll
  for (int x = 1; x < 4; ++x)
#pragma unroll
    for (int jj = 0; jj < 4; ++jj)
      *(LAS f32x2*)(ra[x] + (x - 1) * 2048 + jj * 512) = f32x2{acc[x >> 1][jj][2 * (x & 1)], acc[x >> 1][jj][2 * (x & 1) + 1]};
  meet();
#pragma unroll
  for (int jj = 0; jj < 4; ++jj) {
    f32x2 s = {acc[0][jj][0], acc[0][jj][1]};
#pragma unroll
    for (int x = 1; x < 4; ++x) s += *(LAS f32x2*)(ra[0] + (x - 1) * 2048 + jj * 512);
    r[jj] = s[0]; r[4 + jj] = s[1];
  }
}

#define STR_(x) #x
#define R4(x) "v[" STR_(x) ":" STR_(x) "+3]"
#define R2(x) "v[" STR_(x) ":" STR_(x) "+1]"
#define R1(x) "v[" STR_(x) "]"
#define G_MF(s, j, C) "v_mfma_f32_16x16x32_bf16 " R4(192+4*j) ", " R4(16*s+4*j) ", " R4(160+4*s) ", " C "\n\t"
#define G_S4(s, F0, F1, F2, F3) G_MF(s, 0, R4(192)) F0 G_MF(s, 1, R4(196)) F1 G_MF(s, 2, R4(200)) F2 G_MF(s, 3, R4(204)) F3
#define G_MXB(s) "v_max3_u32 %[mx], %[mx], " R1(160+4*s+2) ", " R1(160+4*s+3) "\n\t"
#define G_MX(s) "v_max3_u32 %[mx], %[mx], " R1(160+4*s) ", " R1(160+4*s+1) "\n\t" G_MXB(s)
#define G_ADD(A, i) "v_pk_add_f32 " R2(A+2*i) ", " R2(A+2*i) ", " R2(192+2*i) "\n\t"
#define G_ADD8(A) G_ADD(A, 0) G_ADD(A, 1) G_ADD(A, 2) G_ADD(A, 3) G_ADD(A, 4) G_ADD(A, 5) G_ADD(A, 6) G_ADD(A, 7)
#define G_W(n) "s_waitcnt vmcnt(" #n ")\n\t"
#define G_LD(s, SO, OFF) "buffer_load_dwordx4 " R4(160+4*s) ", %[vo], %[rs], " SO " offen" OFF " sc1\n\t"
#define G_LD0 G_LD(0, "%[so0]", "")
#define G_LD1 G_LD(1, "%[so0]", " offset:1024")
#define G_LD2 G_LD(2, "%[so0]", " offset:2048")
#define G_LD3 G_LD(3, "%[so0]", " offset:3072")
#define G_LD4 G_LD(4, "%[so1]", "")
#define G_LD5 G_LD(5, "%[so1]", " offset:1024")
#define G_LD6 G_LD(6, "%[so1]", " offset:2048")
#define G_LD7 G_LD(7, "%[so1]", " offset:3072")
#define G_HINT "s_load_dwordx16 s[68:83], %[hp], 0x0 glc\n\t"
#define G_B(i) "s_cmp_eq_u32 s" #i ", %[tag]\n\ts_addc_u32 s85, s85, s85\n\t"
#define G_MASK G_B(83) G_B(82) G_B(81) G_B(80) G_B(79) G_B(78) G_B(77) G_B(76) G_B(75) G_B(74) G_B(73) G_B(72) G_B(71) G_B(70) G_B(69) G_B(68)
#define G_HINT_REGS "s68", "s69", "s70", "s71", "s72", "s73", "s74", "s75", "s76", "s77", "s78", "s79", "s80", "s81", "s82", "s83", "s85"
#define G_RDY "ds_read_b32 %[rdy], %[cnt] offset:%[crdy]\n\t"
#define G_SPIN "s_add_u32 s93, s93, 1\n\ts_cmp_gt_u32 s93, %[cap]\n\ts_cbranch_scc1 99f\n\t"
#define G_FIRST "v_ffbl_b32 %[av], %[vo]\n\tv_lshlrev_b32 %[vo], 15, %[av]\n\t"
#define G_OFFS "v_bitop3_b32 %[vo], %[vo], s92, %[oo] bitop3:0x6a\n\t"
#define G_NXT "v_lshlrev_b32 %[nxt], %[av], 1\n\t"
#define G_LOOP \
  G_HINT "v_mov_b32 %[pend], 15\n\ts_mov_b32 s93, 0\n\ts_mov_b32 s92, 0x40018000\n\t" \
  "s_barrier\n\ts_waitcnt lgkmcnt(0)\n\t" G_MASK \
  "v_bfe_u32 %[vo], s85, %[sh4], 4\n\t" \
  "v_cmp_ne_u32 vcc, 0, %[vo]\n\t" \
  G_FIRST G_NXT G_OFFS \
  "s_barrier\n\t" \
  "s_cbranch_vccnz 81f\n" \
  "90:\n\t" G_RDY G_SPIN \
  "s_waitcnt lgkmcnt(0)\n\t" \
  "v_or_b32 %[rdy], s85, %[rdy]\n\t" \
  "v_bfe_u32 %[vo], %[rdy], %[sh4], 4\n\t" \
  "v_and_b32 %[vo], %[vo], %[pend]\n\t" \
  "v_cmp_ne_u32 vcc, 0, %[vo]\n\t" \
  "s_cbranch_vccz 90b\n\t" \
  G_FIRST G_NXT G_OFFS "\n" \
  "81:\n\t" G_LD0 G_LD1 G_LD2 G_LD3 G_LD4 G_LD5 G_LD6 G_LD7 \
  "v_and_b32 %[cur], %[nxt], %[pend]\n\t" \
  G_W(7) G_RDY "\n" \
  "91:\n\t" G_W(7) \
  G_MF(0, 0, "0") "v_max_u32 %[mx], v160, v161\n\tv_bfi_b32 %[vo], %[cur], 0, %[pend]\n\t" \
  G_MF(0, 1, "0") G_MXB(0) "v_and_b32 %[av], %[t0], %[cur]\n\t" \
  G_MF(0, 2, "0") "v_cmp_ne_u32 s[88:89], 0, %[av]\n\tv_cmp_ne_u32 s[90:91], %[cur], %[av]\n\t" \
  G_MF(0, 3, "0") "s_waitcnt vmcnt(6) lgkmcnt(0)\n\tv_or_b32 %[rdy], s85, %[rdy]\n\t" \
  G_MF(1, 0, R4(192)) "v_bfe_u32 %[av], %[rdy], %[sh4], 4\n\tv_and_b32 %[vo], %[av], %[vo]\n\t" \
  G_MF(1, 1, R4(196)) "v_cmp_ne_u32 vcc, 0, %[vo]\n\t" G_FIRST \
  G_MF(1, 2, R4(200)) G_NXT "s_cbranch_vccz 96f\n\t" G_OFFS \
  G_MF(1, 3, R4(204)) G_LD0 G_W(6) \
  G_S4(2, G_MX(1), G_LD1, G_MX(2), G_LD2 G_W(7)) \
  G_S4(3, G_MX(3), "", "", G_LD3 G_W(7)) \
  G_S4(4, G_MX(4), "", "", G_LD4 G_W(7)) \
  G_S4(5, G_MX(5), "", "", G_LD5 G_W(7)) \
  G_S4(6, G_MX(6), "", "", G_LD6 G_W(7)) \
  G_S4(7, G_MX(7), "v_cmp_eq_u32 vcc, -1, %[mx]\n\t", G_RDY, G_LD7) \
  "s_branch 97f\n" \
  "95:\n\t" G_SPIN \
  "s_or_b32 s86, vcc_lo, vcc_hi\n\t" \
  "s_lshr_b32 s87, s86, 16\n\ts_or_b32 s86, s86, s87\n\ts_lshr_b32 s87, s86, 2\n\ts_or_b32 s86, s86, s87\n\ts_lshr_b32 s87, s86, 1\n\ts_or_b32 s86, s86, s87\n\t" \
  "s_and_b32 s86, s86, 0x1111\n\ts_mul_i32 s86, s86, 0xf000f\n\ts_mov_b32 s87, s86\n\t" \
  "s_andn2_b64 s[88:89], s[88:89], s[86:87]\n\ts_andn2_b64 s[90:91], s[90:91], s[86:87]\n\t" \
  "s_branch 92f\n" \
  "96:\n\t" \
  G_MF(1, 3, R4(204)) G_W(5) \
  G_S4(2, G_MX(1), "", G_MX(2), G_W(4)) \
  G_S4(3, G_MX(3), "", "", G_W(3)) \
  G_S4(4, G_MX(4), "", "", G_W(2)) \
  G_S4(5, G_MX(5), "", "", G_W(1)) \
  G_S4(6, G_MX(6), "", "", G_W(0)) \
  G_S4(7, G_MX(7), "v_cmp_eq_u32 vcc, -1, %[mx]\n\t", "s_nop 0\n\t", "\n") \
  "97:\n\t" \
  "s_cbranch_vccnz 95b\n" \
  "92:\n\t" \
  "s_mov_b64 exec, s[88:89]\n\t" \
  "s_cbranch_execz 93f\n\t" \
  "v_xor_b32 %[pend], %[pend], %[cur]\n\t" G_ADD8(128) "\n" \
  "93:\n\t" \
  "s_mov_b64 exec, s[90:91]\n\t" \
  "s_cbranch_execz 94f\n\t" \
  "v_xor_b32 %[pend], %[pend], %[cur]\n\t" G_ADD8(144) "\n" \
  "94:\n\t" \
  "s_mov_b64 exec, -1\n\t" \
  "v_and_b32 %[cur], %[nxt], %[pend]\n\t" \
  "v_cmp_ne_u32 vcc, 0, %[cur]\n\t" \
  "s_cbranch_vccnz 91b\n\t" \
  "v_cmp_ne_u32 vcc, 0, %[pend]\n\t" \
  "s_cbranch_vccnz 90b\n" \
  "99:\n\ts_waitcnt vmcnt(0) lgkmcnt(0)"

__global__ __launch_bounds__(512, 2) void gqa_fused(const char* __restrict__ x, const char* __restrict__ kc, const char* __restrict__ vc,
                                                    const char* __restrict__ wq, const char* __restrict__ wo, char* __restrict__ out, char* __restrict__ ws) {
  extern __shared__ __attribute__((aligned(16))) char smem[];
  LAS char* L = (LAS char*)smem;
  const int tid = threadIdx.x, lane = tid & 63, wave = __builtin_amdgcn_readfirstlane(tid >> 6);
  const int wg = blockIdx.x;
  i32x4 rcode;
  {
    u64 pc; int code_len;
    asm volatile("s_getpc_b64 %0\n.Lgqa_pc:\n\ts_mov_b32 %1, .Lgqa_end-.Lgqa_pc" : "=s"(pc), "=s"(code_len));
    rcode = mkrs((const void*)pc, code_len);
    touch_lines(rcode, lane * 128 + wave * 65536, (wg & 24) * 1024);
    SB();
    const i32x4 rx = mkrs(x, 262144), rq = mkrs(wq, 8 << 20);
    const int row = (wg >> 3) * 8 + wave, qrow = row * 32768 + (wg & 7) * 1024;
    asm volatile("s_mov_b32 m0, %8\n\ts_nop 0\n\t"
                 "buffer_load_dwordx4 %0, %1, %2 offen lds\n\tbuffer_load_dwordx4 %0, %1, %3 offen lds\n\tbuffer_load_dwordx4 %0, %1, %4 offen lds\n\t"
                 "buffer_load_dwordx4 %0, %1, %5 offen lds\n\tbuffer_load_dwordx4 %0, %6, %7 offen lds"
                 :: "v"(lane * 16), "s"(rq), "s"(qrow), "s"(qrow + 8192), "s"(qrow + 16384), "s"(qrow + 24576), "s"(rx), "s"(row * 1024), "n"(L_SINK),
                    "s"(kc), "s"(vc), "s"(wo), "s"(out), "s"(ws) : "memory");
    SB();
  }
  *((LAS unsigned*)(L + L_CNT) + (lane & 15)) = 0u;
  asm volatile("s_waitcnt lgkmcnt(0)\n\ts_barrier" ::: "memory");

  const int c = lane & 15, g = lane >> 4;
  const int b = wg >> 3, hkv = (wg >> 1) & 3, half = wg & 1, rg = wg >> 6, i8 = b & 7;
  const int grp = rg * 4 + hkv;
  const int oslot = WS_O + ((grp * 2 + (i8 >> 2)) * 2 + half) * 32768 + (i8 & 3) * 256;
  const char* hp = ws + WS_HINT + wg * 128;
  const __amdgpu_buffer_rsrc_t rws = __builtin_amdgcn_make_buffer_rsrc((void*)ws, 0, WS_BYTES, 0x00020000);

  if (wave >= 4) {
    const int v = wave - 4, tv = v >> 1, hf = v & 1;
    __builtin_amdgcn_s_setprio(3);
    const unsigned tstart = now();
    const unsigned epoch = __builtin_amdgcn_raw_buffer_load_b32(rws, 0, WS_E, SC1);
#pragma unroll
    for (int e = 0; e < 2; ++e) st_dev(rws, oslot + v * 1024 + (e * 4 + g) * 4096 + c * 16, u32x4{~0u, ~0u, ~0u, ~0u});
    const int wcol = ((lane & 7) ^ (2 * hf)) * 16, wb = (lane >> 3) & 1;
    f32x4 acc[2][4];
    const int ct = (wg & 7) * 8 + i8;
    {
      const __amdgpu_buffer_rsrc_t rq = __builtin_amdgcn_make_buffer_rsrc((void*)(wq + (size_t)v * 256 * 8192 + ct * 128), 0, 256 * 8192 - ct * 128, 0x00020000);
      const __amdgpu_buffer_rsrc_t rx = __builtin_amdgcn_make_buffer_rsrc((void*)(x + rg * 65536), 0, 65536, 0x00020000);
      const int wv = (g * 8 + wb) * 8192 + wcol, xv0 = (tv * 16 + (lane >> 2)) * 2048 + v * 512 + (lane & 3) * 16, xv1 = xv0 ^ 32768;
      const int xsel = (4 * c + g) * 4;
      u32x4 wr[8][4], xr[8][2];
      until(tstart + QT0 + b * QT1);
#pragma unroll
      for (int s = 0; s < 8; ++s) {
        if (!(s & 1)) {
#pragma unroll
          for (int i = 0; i < 4; ++i) xr[s + (i & 1)][i >> 1] = __builtin_amdgcn_raw_buffer_load_b128(rx, ((i >> 1) ? xv1 : xv0) + (s + (i & 1)) * 64, 0, 0);
        }
#pragma unroll
        for (int i = 0; i < 4; ++i) wr[s][i] = __builtin_amdgcn_raw_buffer_load_b128(rq, wv, (s * 32 + 2 * i) * 8192, 0);
      }
      SB();
#pragma unroll
      for (int s = 0; s < 8; ++s) {
        u32x4 xf[2], a[4];
#pragma unroll
        for (int t = 0; t < 2; ++t)
#pragma unroll
          for (int u = 0; u < 4; ++u) xf[t][u] = __builtin_amdgcn_ds_bpermute(xsel, xr[s][t][u]);
        wtrans(wr[s], a);
#pragma unroll
        for (int jj = 0; jj < 4; ++jj)
#pragma unroll
          for (int t = 0; t < 2; ++t) acc[t][jj] = mfma16(a[jj], xf[t], s ? acc[t][jj] : f32x4{0.f, 0.f, 0.f, 0.f});
      }
    }
    const unsigned tag = __builtin_amdgcn_readfirstlane(epoch) + 1;
    LAS char* ra[4];
#pragma unroll
    for (int xw = 0; xw < 4; ++xw) { ra[xw] = L + L_SCR + (v ^ xw) * 6144 + lane * 8; asm volatile("" : "+v"(ra[xw])); }
    {
      float r[8];
      proj_reduce(ra, acc, r, [] { lds_signal<C_QRED>(); lds_spin<C_QRED, 4>(); });
      const auto s0 = __builtin_amdgcn_permlane32_swap(cvtpk(r[0], r[1]), cvtpk(r[4], r[5]), false, false);
      const auto s1 = __builtin_amdgcn_permlane32_swap(cvtpk(r[2], r[3]), cvtpk(r[6], r[7]), false, false);
      st_dev(rws, WS_Q + ((rg * 8 + tv * 4 + (c >> 2)) * 4 + hkv) * 8192 + (ct & 15) * 512 + hf * 256 + g * 64 + (c & 3) * 16, u32x4{s0[0], s1[0], s0[1], s1[1]});
      wait_vm<0>();
      const unsigned before = __builtin_amdgcn_readfirstlane(
          lane == 0 ? __hip_atomic_fetch_add((LAS unsigned*)(L + L_CNT) + C_QST, 1u, __ATOMIC_RELAXED, __HIP_MEMORY_SCOPE_WORKGROUP) : 0u);
      MEMBAR();
      if (before == 3) __builtin_amdgcn_raw_buffer_store_b32(tag, rws, lane < 3 ? WS_FLAG + (grp * 4 + lane) * 128 + (i8 * 2 + half) * 4 : FAR, 0, SC1);
    }
    const int cg = half * 8 + i8;
    u32x4 wr[8][4];
    {
      const __amdgpu_buffer_rsrc_t ro = __builtin_amdgcn_make_buffer_rsrc((void*)(wo + (size_t)(hkv * 1024 + v * 256) * 2048 + cg * 128), 0, 256 * 2048 - cg * 128, 0x00020000);
      const int wv = (g * 4 + wb) * 2048 + wcol;
      until(tstart + OT0 + b * OT1);
#pragma unroll
      for (int s = 0; s < 8; ++s)
#pragma unroll
        for (int i = 0; i < 4; ++i) wr[s][i] = __builtin_amdgcn_raw_buffer_load_b128(ro, wv, ((s ^ (i8 & 4)) * 32 + (i >> 1) * 16 + (i & 1) * 2) * 2048, 0);
      SB();
    }
    {
      int qo = WS_Q + (wg >> 1) * 8192 + tv * 4096 + hf * 512 + (c >> 2) * 1024 + (g & 1) * 256 + (g >> 1) * 64 + (c & 3) * 16;
      asm volatile("" : "+v"(qo));
      if (v == 0) {
        await_hint<16>(ws + WS_FLAG + (grp * 4 + 1 + half) * 128, tag);
        _Pragma("nounroll") for (unsigned spin = 0; spin < SPIN_CAP; ++spin) {
          MEMBAR();
          if (__all(__builtin_amdgcn_raw_buffer_load_b32(rws, WS_FLAG + grp * 512 + c * 4, 0, SC1) == tag)) break;
        }
        *((LAS unsigned*)(L + L_CNT) + C_TAG) = tag;
        lds_signal<C_FLAG>();
      } else {
        lds_wait<C_FLAG, 1>();
      }
      u32x4 d[2];
#pragma unroll
      for (int e = 0; e < 2; ++e) d[e] = __builtin_amdgcn_raw_buffer_load_b128(rws, qo + e * 128, 0, SC1);
#pragma unroll
      for (int e = 0; e < 2; ++e) *(LAS u32x4*)(L + L_SCR + v * 6144 + e * 1024 + lane * 16) = d[e];
      lds_signal<C_QRDY>();
    }
    __builtin_amdgcn_s_setprio(0);
    u32x32 wf0, wf1, wf2, wf3;
    {
      auto pack = [&](int s2, u32x32& o) {
#pragma unroll
        for (int e = 0; e < 2; ++e) {
          u32x4 a[4];
          wtrans(wr[s2 + e], a);
#pragma unroll
          for (int i = 0; i < 16; ++i) o[e * 16 + i] = a[i >> 2][i & 3];
        }
      };
      pack(0, wf0); asm volatile("" : "+{v[0:31]}"(wf0)); SB();
      pack(2, wf1); asm volatile("" : "+{v[32:63]}"(wf1)); SB();
      pack(4, wf2); asm volatile("" : "+{v[64:95]}"(wf2)); SB();
      pack(6, wf3); asm volatile("" : "+{v[96:127]}"(wf3)); SB();
    }
    __builtin_amdgcn_s_setprio(3);
    if (v == 0) {
      lds_wait<C_ML, 4>();
      const int qi = lane & 31;
      f32x2 ml[4];
#pragma unroll
      for (int i = 0; i < 4; ++i) ml[i] = *(LAS f32x2*)(L + L_ML + (i * 32 + qi) * 8);
      const float M = fmaxf(fmaxf(ml[0][0], ml[1][0]), fmaxf(ml[2][0], ml[3][0]));
      float Ls = 0.f;
#pragma unroll
      for (int i = 0; i < 4; ++i) Ls += ml[i][1] * __builtin_amdgcn_exp2f(ml[i][0] - M);
      st_dev(rws, lane < 32 ? WS_ML + wg * 512 + qi * 16 : FAR, u32x4{__float_as_uint(M), tag, __float_as_uint(Ls), tag});
      lds_wait<C_MLRD, 1>();
      const char* mp = ws + WS_ML + (wg ^ 1) * 512;
      _Pragma("nounroll") for (int spin = 0; spin < 32; ++spin) {
        unsigned y0, y1;
        asm volatile("s_load_dword %0, %2, 0x4 glc\n\ts_load_dword %1, %2, 0x1fc glc\n\ts_waitcnt lgkmcnt(0)" : "=&s"(y0), "=&s"(y1) : "s"(mp) : "memory");
        if (y0 == tag && y1 == tag) break;
      }
      u32x4 d;
      _Pragma("nounroll") for (unsigned spin = 0; spin < SPIN_CAP; ++spin) {
        MEMBAR();
        d = __builtin_amdgcn_raw_buffer_load_b128(rws, WS_ML + (wg ^ 1) * 512 + qi * 16, 0, SC1);
        if (__all(d[1] == tag && d[3] == tag)) break;
        __builtin_amdgcn_s_sleep(8);
      }
      const float M2 = __uint_as_float(d[0]), L2 = __uint_as_float(d[2]);
      const float Mt = fmaxf(M, M2);
      const float inv = 1.f / (Ls * __builtin_amdgcn_exp2f(M - Mt) + L2 * __builtin_amdgcn_exp2f(M2 - Mt));
#pragma unroll
      for (int i = 0; i < 4; ++i) *(LAS float*)(L + L_F + (i * 32 + qi) * 4) = __builtin_amdgcn_exp2f(ml[i][0] - Mt) * inv;
      lds_signal<C_F>();
    } else if (v == 1) {
      touch_lines(rcode, lane * 128, (wg & 24) * 1024);
    } else if (v == 3 && wg == 0) {
      lds_wait<C_ML, 4>();
      _Pragma("nounroll") for (unsigned spin = 0; spin < SPIN_CAP; ++spin) {
        MEMBAR();
        const u32x4 fl = __builtin_amdgcn_raw_buffer_load_b128(rws, WS_FLAG + (lane >> 2) * 512 + (lane & 3) * 16, 0, SC1);
        if (__all(((fl[0] ^ tag) | (fl[1] ^ tag) | (fl[2] ^ tag) | (fl[3] ^ tag)) == 0)) break;
        __builtin_amdgcn_s_sleep(16);
      }
      __builtin_amdgcn_raw_buffer_store_b32(tag, rws, lane == 0 ? WS_E : FAR, 0, SC1);
    }
    __builtin_amdgcn_s_setprio(0);
    int pst = WS_P + wg * 8192 + tv * 4096 + hf * 1024 + (c >> 3) * 2048 + g * 128 + (c & 7) * 16;
    int pld = WS_P + (wg & ~6) * 8192 + hkv * 2048 + v * 512 + lane * 8;
    int pho = (v == 0 && lane < 4) ? WS_PH + ((wg & ~6) + lane * 2) * 128 + hkv * 4 : FAR;
    int ost = (rg * 32 + hkv * 8 + (c >> 1)) * 2048 + cg * 128 + (g & 1) * 64 + v * 16 + (g >> 1) * 8 + (c & 1) * 4;
    {
      const int rot = i8 & 3;
      int oo = (c >> 2) * 256 + g * 64 + (c & 3) * 16 + rot * 32768, sh4 = c & 12;
      unsigned cnt = L_CNT;
      i32x4 rs = mkrs(ws, WS_BYTES);
      int so0 = WS_O + grp * 131072 + v * 8192 + (i8 & 4) * 1024, so1 = so0 ^ 4096, t0 = ((rot >> 1) ^ tv) ? 12 : 3;
      f32x32 res = 0.f;
      asm volatile("" : "+v"(oo), "+v"(sh4), "+v"(cnt), "+v"(ra[0]), "+v"(ra[1]), "+v"(ra[2]), "+v"(ra[3]), "+v"(pst), "+v"(pld), "+v"(ost), "+v"(pho),
                        "+{v[128:159]}"(res), "+s"(rs), "+s"(so0), "+s"(so1), "+s"(t0), "+s"(hp));
      lds_wait<C_LAST, 4>();
      u32x32 buf;
      unsigned vo, mx, av, pend, cur, nxt, rdy;
      asm volatile(
          G_LOOP
          : "+{v[128:159]}"(res), "=&{v[160:191]}"(buf), [vo] "=&v"(vo), [mx] "=&v"(mx), [av] "=&v"(av), [pend] "=&v"(pend), [cur] "=&v"(cur),
            [nxt] "=&v"(nxt), [rdy] "=&v"(rdy)
          : "{v[0:31]}"(wf0), "{v[32:63]}"(wf1), "{v[64:95]}"(wf2), "{v[96:127]}"(wf3), [oo] "v"(oo), [sh4] "v"(sh4), [cnt] "v"(cnt), [hp] "s"(hp),
            [rs] "s"(rs), [so0] "s"(so0), [so1] "s"(so1), [t0] "s"(t0), [tag] "s"(tag), [cap] "n"(SPIN_CAP * 16), [crdy] "n"(4 * C_RDY)
          : "memory", "vcc", "scc", G_HINT_REGS, "s86", "s87", "s88", "s89", "s90", "s91", "s92", "s93", "v192", "v193", "v194", "v195", "v196", "v197",
            "v198", "v199", "v200", "v201", "v202", "v203", "v204", "v205", "v206", "v207");
#pragma unroll
      for (int i = 0; i < 32; ++i) acc[i >> 4][(i >> 2) & 3][i & 3] = res[i];
    }
    {
      float r[8];
      __builtin_amdgcn_raw_buffer_store_b32(tag, rws, pho, 0, SC1);
      proj_reduce(ra, acc, r, [] { asm volatile("s_waitcnt lgkmcnt(0)\n\ts_barrier" ::: "memory"); });
#pragma unroll
      for (int e = 0; e < 2; ++e)
        st_dev(rws, pst + e * 512, u32x4{tag8(r[4 * e], tag), tag8(r[4 * e + 1], tag), tag8(r[4 * e + 2], tag), tag8(r[4 * e + 3], tag)});
    }
    await_hint<4>(ws + WS_PH + wg * 128, tag);
    {
      u32x2 d[4];
      _Pragma("nounroll") for (unsigned spin = 0; spin < SPIN_CAP; ++spin) {
        MEMBAR();
#pragma unroll
        for (int i = 0; i < 4; ++i) d[i] = __builtin_amdgcn_raw_buffer_load_b64(rws, pld, i * 16384, SC1);
        unsigned mx = 0;
#pragma unroll
        for (int i = 0; i < 4; ++i) { d[i] ^= tag & 0xFFu; mx |= d[i][0] | d[i][1]; }
        if (__all((mx & 0xFFu) == 0)) break;
      }
      const f32x2 s = (__builtin_bit_cast(f32x2, d[0]) + __builtin_bit_cast(f32x2, d[1])) + (__builtin_bit_cast(f32x2, d[2]) + __builtin_bit_cast(f32x2, d[3]));
      __builtin_amdgcn_raw_buffer_store_b32(cvtpk(s[0], s[1]), __builtin_amdgcn_make_buffer_rsrc((void*)out, 0, 262144, 0x00020000), ost, 0, 0);
    }
    asm volatile(".Lgqa_end:");
    __builtin_amdgcn_endpgm();
  } else {
    const int w = wave;
    const i32x4 rk = mkrs(kc + (size_t)wg * 262144, 262144), rv = mkrs(vc + (size_t)wg * 262144, 262144);
    const unsigned ring = w * 32768;
    int kv[4], vv[4];
#pragma unroll
    for (int k = 0; k < 4; ++k) {
      const int o = k * 3072 + w * 1024 + g * 256;
      kv[k] = o + (c ^ (4 * k + g)) * 16;
      vv[k] = o + (c ^ (2 * (4 * (k & 1) + g))) * 16;
    }
    auto issue = [&](auto jc) {
      constexpr int j = decltype(jc)::value;
      if constexpr (j < 16) dma_tile<WIN1>(rk, ring + (j & 7) * 4096, j * 16384, kv);
      else dma_tile<(j < 20 ? WIN1 : WIN2)>(rv, ring + (j & 7) * 4096, (j - 16) * 16384, vv);
    };
    LAS char* ka[4];
#pragma unroll
    for (int s = 0; s < 4; ++s) ka[s] = L + ring + c * 256 + (((2 * g) ^ c) ^ (8 * (s >> 1) + (s & 1))) * 16;
    {
      const i32x4 ro = mkrs(wo, 8 << 20);
#pragma unroll
      for (int i = 0; i < 8; ++i)
        asm volatile("s_mov_b32 m0, %3\n\ts_waitcnt vmcnt(%4)\n\tbuffer_load_dwordx4 %0, %1, %2 offen lds"
                     :: "v"(lane * 16), "s"(ro), "s"((hkv * 1024 + b * 32 + w * 8 + i) * 2048 + half * 1024), "n"(L_SINK), "n"(WINP - 1) : "memory");
    }
    u32x4 kr[12][4];
    sfor<0, 20>([&](auto jc) {
      constexpr int j = decltype(jc)::value, t = j - WIN1 / 4;
      if constexpr (j >= 14) lds_spin<C_STEP, 4 * (j - 12) - 1>();
      issue(jc);
      if constexpr (j >= 12 && j < 19) lds_bump<C_STEP>();
      if constexpr (t >= 0 && t < 12) {
#pragma unroll
        for (int s = 0; s < 4; ++s) kr[t][s] = *(LAS u32x4*)(ka[s] + (t & 7) * 4096);
      }
    });
    lds_wait<C_QRDY, 4>();
    u32x4 q[2][4];
#pragma unroll
    for (int f = 0; f < 8; ++f) q[f >> 2][f & 3] = *(LAS u32x4*)(L + L_SCR + (f >> 1) * 6144 + (f & 1) * 1024 + lane * 16);
    f32x4 sc[16][2];
    float m[2] = {-__builtin_inff(), -__builtin_inff()};
    auto qk = [&](const u32x4 (&kf)[4], f32x4 (&o)[2]) {
#pragma unroll
      for (int qt = 0; qt < 2; ++qt) {
        f32x4 a = {0.f, 0.f, 0.f, 0.f};
#pragma unroll
        for (int s = 0; s < 4; ++s) a = mfma16(kf[s], q[qt][s], a);
        o[qt] = a;
      }
    };
    auto vmax = [&](const f32x4 (&t)[2]) {
#pragma unroll
      for (int qt = 0; qt < 2; ++qt) m[qt] = fmaxf(fmaxf(fmaxf(fmaxf(m[qt], t[qt][0]), t[qt][1]), t[qt][2]), t[qt][3]);
    };
    qk(kr[0], sc[0]);
    qk(kr[1], sc[1]);
    SB();
    sfor<0, 4>([&](auto nc) {
      constexpr int n = decltype(nc)::value;
      wait_vm<28>();
      u32x4 kf[4];
#pragma unroll
      for (int s = 0; s < 4; ++s) kf[s] = *(LAS u32x4*)(ka[s] + ((12 + n) & 7) * 4096);
      qk(kr[2 + n], sc[2 + n]);
      vmax(sc[n ? 11 + n : 0]);
      if constexpr (n < 3) issue(ic<20 + n>{});
      qk(kf, sc[12 + n]);
      vmax(sc[1 + n]);
      SB();
    });
    sfor<6, 12>([&](auto tc) {
      constexpr int t = decltype(tc)::value;
      qk(kr[t], sc[t]);
      vmax(sc[t - 1]);
      if constexpr (t == 6) vmax(sc[15]);
      SB();
    });
    issue(ic<23>{});
    vmax(sc[11]);
#pragma unroll
    for (int qt = 0; qt < 2; ++qt) m[qt] = xg_max(m[qt]) * C2;
    u32x4 p[8][2];
    f32x2 l[2] = {{0.f, 0.f}, {0.f, 0.f}};
    auto probs = [&](auto ec) {
      constexpr int e = decltype(ec)::value;
#pragma unroll
      for (int qt = 0; qt < 2; ++qt) {
#pragma unroll
        for (int j = 0; j < 4; ++j) {
          const f32x4 t = sc[2 * e + (j >> 1)][qt];
          const f32x2 a = __builtin_elementwise_fma(f32x2{t[2 * (j & 1)], t[2 * (j & 1) + 1]}, f32x2{C2, C2}, f32x2{-m[qt], -m[qt]});
          const f32x2 ex = {__builtin_amdgcn_exp2f(a[0]), __builtin_amdgcn_exp2f(a[1])};
          l[qt] += ex;
          p[e][qt][j] = cvtpk(ex[0], ex[1]);
        }
        asm volatile("" : "+v"(p[e][qt]));
      }
    };
    LAS char* va[8];
#pragma unroll
    for (int a = 0; a < 8; ++a) va[a] = L + ring + (lane >> 2) * 256 + ((a ^ (2 * w)) ^ ((lane >> 2) & 7)) * 32 + (lane & 3) * 8;
    f32x4 o[8][2];
    float f[4][2];
    auto read_f = [&] {
#pragma unroll
      for (int xw = 0; xw < 4; ++xw)
#pragma unroll
        for (int qt = 0; qt < 2; ++qt) f[xw][qt] = *(LAS float*)(L + L_F + c * 4 + ((w ^ xw) * 32 + qt * 16) * 4);
      asm volatile("" : "+v"(f[0][0]), "+v"(f[0][1]), "+v"(f[1][0]), "+v"(f[1][1]), "+v"(f[2][0]), "+v"(f[2][1]), "+v"(f[3][0]), "+v"(f[3][1]));
    };
    int ooff[2], have_f;
    ooff[0] = oslot + w * 1024 + (c >> 2) * 4096 + g * 64 + (c & 3) * 16;
    ooff[1] = ooff[0] + 16384;
    int ho = lane < 16 ? WS_HINT + ((wg & 0xC6) | ((c >> 1) << 3) | (c & 1)) * 128 + ((i8 & 3) * 4 + ((((i8 >> 2) * 2) | half) ^ ((c >> 1) & 3))) * 4 : FAR;
    unsigned tg;
    LAS char* rd[4];
#pragma unroll
    for (int xw = 0; xw < 4; ++xw) rd[xw] = L + (w ^ xw) * 32768 + lane * 16;
    sfor<0, 8>([&](auto bc) {
      constexpr int bl = decltype(bc)::value, issued = bl < 4 ? 24 + 2 * bl : 32;
      if constexpr (bl < 2) sfor<4 * bl, 4 * bl + 4>(probs);
      if constexpr (bl == 1) {
#pragma unroll
        for (int qt = 0; qt < 2; ++qt) {
          const float ls = xg_sum(l[qt][0] + l[qt][1]);
          if (g == 0) *(LAS f32x2*)(L + L_ML + (w * 32 + qt * 16 + c) * 8) = f32x2{m[qt], ls};
        }
        lds_signal<C_ML>();
      }
      SB();
      if constexpr (bl == 7) {
        MEMBAR();
        have_f = __builtin_amdgcn_readfirstlane(*(volatile LAS unsigned*)((LAS unsigned*)(L + L_CNT) + C_F));
        MEMBAR();
        read_f();
        tg = *((LAS unsigned*)(L + L_CNT) + C_TAG);
        asm volatile("" : "+v"(ooff[0]), "+v"(ooff[1]), "+v"(rd[0]), "+v"(rd[1]), "+v"(rd[2]), "+v"(rd[3]), "+v"(tg), "+v"(ho));
      }
      s16x4 vf[2][8];
      sfor<0, 2>([&](auto tc) {
        constexpr int t = decltype(tc)::value, j = 16 + 2 * bl + t;
        wait_vm<4 * (issued - 1 - j)>();
#pragma unroll
        for (int a = 7; a >= 0; --a) vf[t][a] = __builtin_amdgcn_ds_read_tr16_b64_v4i16((LAS s16x4*)(va[a] + (j & 7) * 4096));
      });
      if constexpr (bl == 7) { lds_bump<C_LAST>(); SB(); }
#pragma unroll
      for (int a = 7; a >= 0; --a) {
        const u32x2 lo = __builtin_bit_cast(u32x2, vf[0][a]), hi = __builtin_bit_cast(u32x2, vf[1][a]);
        const u32x4 af = {lo[0], lo[1], hi[0], hi[1]};
#pragma unroll
        for (int qt = 0; qt < 2; ++qt) {
          o[a][qt] = mfma16(af, p[bl][qt], bl ? o[a][qt] : f32x4{0.f, 0.f, 0.f, 0.f});
          if constexpr (bl == 7) { if (a < 6) *(LAS f32x4*)(rd[0] + ((a + 2) * 2 + qt) * 1024) = o[a + 2][qt]; SB(); }
        }
      }
      SB();
      if constexpr (bl < 4) {
        issue(ic<issued>{});
        if constexpr (bl == 2) lds_bump<C_MLRD>();
        issue(ic<issued + 1>{});
        if constexpr (bl == 3) __builtin_amdgcn_s_setprio(3);
      }
    });
    if (!have_f) { lds_spin<C_F, 1>(); read_f(); }
    f32x4 sum[2][2];
#pragma unroll
    for (int i = 0; i < 4; ++i) { sum[i >> 1][i & 1] = o[i & 1][i >> 1] * f[0][i >> 1]; asm volatile("" : "+v"(sum[i >> 1][i & 1])); }
    asm volatile("s_waitcnt lgkmcnt(0)\n\ts_barrier" ::: "memory");
    if (w == 0) __builtin_amdgcn_raw_buffer_store_b32(tg, rws, ho, 0, SC1);
    f32x4 part[2][3][2];
#pragma unroll
    for (int qt = 0; qt < 2; ++qt) {
#pragma unroll
      for (int xw = 1; xw < 4; ++xw)
#pragma unroll
        for (int e = 0; e < 2; ++e) part[qt][xw - 1][e] = *(LAS f32x4*)(rd[xw] + (xw * 4 + 2 * e + qt) * 1024);
      SB();
    }
#pragma unroll
    for (int qt = 0; qt < 2; ++qt) {
#pragma unroll
      for (int xw = 1; xw < 4; ++xw)
#pragma unroll
        for (int e = 0; e < 2; ++e) sum[qt][e] = __builtin_elementwise_fma(part[qt][xw - 1][e], f32x4{f[xw][qt], f[xw][qt], f[xw][qt], f[xw][qt]}, sum[qt][e]);
      const f32x4 s0 = sum[qt][0], s1 = sum[qt][1];
      st_dev(rws, ooff[qt], u32x4{nomark(cvtpk(s0[0], s0[1])), nomark(cvtpk(s0[2], s0[3])), nomark(cvtpk(s1[0], s1[1])), nomark(cvtpk(s1[2], s1[3]))});
      SB();
    }
    {
      const unsigned tag = __builtin_amdgcn_readfirstlane(tg);
      unsigned t;
      asm volatile(G_HINT "s_barrier\n\t"
                   "s_cmp_lt_u32 %[w], 2\n\ts_cbranch_scc1 73f\n\t"
                   "s_mov_b32 s84, 64\n\ts_mov_b64 exec, 1\n\t"
                   "s_cmp_eq_u32 %[w], 3\n\ts_cbranch_scc1 71f\n\t"
                   "s_waitcnt lgkmcnt(0)\n\ts_sleep 3\n"
                   "70:\n\t" G_HINT "\n"
                   "71:\n\ts_waitcnt lgkmcnt(0)\n\t" G_MASK
                   "s_sub_u32 s84, s84, 1\n\ts_cselect_b32 s85, -1, s85\n\t"
                   "v_mov_b32 %[t], s85\n\tds_or_b32 %[cnt], %[t] offset:%[crdy]\n\t"
                   "s_andn2_b32 s85, 0xffff, s85\n\ts_cbranch_scc1 70b\n"
                   "73:\n\ts_mov_b64 exec, -1\n\ts_waitcnt lgkmcnt(0)"
                   : [t] "=&v"(t) : [hp] "s"(hp), [tag] "s"(tag), [w] "s"(w), [cnt] "v"((unsigned)L_CNT), [crdy] "n"(4 * C_RDY)
                   : "memory", "scc", G_HINT_REGS, "s84");
    }
    __builtin_amdgcn_endpgm();
  }
}

extern "C" void kernel_launch(void* const* d_in, const int* in_sizes, int n_in, void* d_out, int out_size, void* d_ws, size_t ws_size,
                              hipStream_t stream) {
  (void)hipFuncSetAttribute((const void*)gqa_fused, hipFuncAttributeMaxDynamicSharedMemorySize, LDS_BYTES);
  gqa_fused<<<256, 512, LDS_BYTES, stream>>>((const char*)d_in[0], (const char*)d_in[1], (const char*)d_in[2], (const char*)d_in[3],
                                             (const char*)d_in[4], (char*)d_out, (char*)d_ws);
}
```

```cpp
#include <hip/hip_runtime.h>
#include <stdint.h>

typedef unsigned u32x2 __attribute__((ext_vector_type(2)));
typedef unsigned u32x4 __attribute__((ext_vector_type(4)));
typedef unsigned u32x16 __attribute__((ext_vector_type(16)));
typedef unsigned u32x32 __attribute__((ext_vector_type(32)));
typedef int i32x4 __attribute__((ext_vector_type(4)));
typedef float f32x2 __attribute__((ext_vector_type(2)));
typedef float f32x4 __attribute__((ext_vector_type(4)));
typedef float f32x32 __attribute__((ext_vector_type(32)));
typedef short bf16x8 __attribute__((ext_vector_type(8)));
typedef short s16x4 __attribute__((ext_vector_type(4)));
typedef unsigned long long u64;
#define LAS __attribute__((address_space(3)))
#define SB() __builtin_amdgcn_sched_barrier(0)
#define MEMBAR() asm volatile("" ::: "memory")

constexpr int WIN1 = 8, WIN2 = 13;
constexpr int WINP = 6;
constexpr int QT0 = 210, QT1 = 17;

constexpr int WS_E = 0, WS_ML = 8192, WS_FLAG = 192 << 10, WS_HINT = 256 << 10, WS_PH = 320 << 10, WS_Q = 1 << 20, WS_O = 4 << 20, WS_P = 8 << 20,
              WS_BYTES = 10 << 20;
constexpr int L_SCR = 131072, L_ML = L_SCR + 24576, L_F = L_ML + 1024, L_CNT = L_F + 512, L_SINK = L_CNT + 128, LDS_BYTES = L_SINK + 1024;
enum { C_QRED = 0, C_QST, C_FLAG, C_QRDY, C_STEP, C_ML, C_MLRD, C_F, C_LAST, C_RDY, C_TAG };
constexpr int SC1 = 16;
constexpr int FAR = 0x7F000000;
constexpr unsigned SPIN_CAP = 1u << 16;
constexpr float C2 = 0.088388347648318440f * 1.4426950408889634f;

template <int N> using ic = std::integral_constant<int, N>;
template <int I, int N, class F> __device__ __forceinline__ void sfor(F&& f) {
  if constexpr (I < N) { f(ic<I>{}); sfor<I + 1, N>(f); }
}
__device__ __forceinline__ f32x4 mfma16(u32x4 a, u32x4 b, f32x4 c) {
  return __builtin_amdgcn_mfma_f32_16x16x32_bf16(__builtin_bit_cast(bf16x8, a), __builtin_bit_cast(bf16x8, b), c, 0, 0, 0);
}
__device__ __forceinline__ unsigned cvtpk(float lo, float hi) {
  typedef __bf16 b2 __attribute__((ext_vector_type(2)));
  return __builtin_bit_cast(unsigned, __builtin_convertvector(f32x2{lo, hi}, b2));
}
__device__ __forceinline__ float xg_max(float x) {
  auto s = __builtin_amdgcn_permlane16_swap(__float_as_uint(x), __float_as_uint(x), false, false);
  x = fmaxf(__uint_as_float(s[0]), __uint_as_float(s[1]));
  auto t = __builtin_amdgcn_permlane32_swap(__float_as_uint(x), __float_as_uint(x), false, false);
  return fmaxf(__uint_as_float(t[0]), __uint_as_float(t[1]));
}
__device__ __forceinline__ float xg_sum(float x) {
  auto s = __builtin_amdgcn_permlane16_swap(__float_as_uint(x), __float_as_uint(x), false, false);
  x = __uint_as_float(s[0]) + __uint_as_float(s[1]);
  auto t = __builtin_amdgcn_permlane32_swap(__float_as_uint(x), __float_as_uint(x), false, false);
  return __uint_as_float(t[0]) + __uint_as_float(t[1]);
}
__device__ __forceinline__ unsigned tag8(float x, unsigned t) { return (__float_as_uint(x) & ~0xFFu) | (t & 0xFFu); }
__device__ __forceinline__ unsigned nomark(unsigned x) { return min(x, 0xFFFFFFFEu); }

template <int WHICH> __device__ __forceinline__ void lds_signal() {
  asm volatile("s_mov_b64 exec, 1\n\ts_waitcnt lgkmcnt(0)\n\tds_add_u32 %0, %1 offset:%2\n\ts_mov_b64 exec, -1"
               :: "v"((unsigned)L_CNT), "v"(1u), "n"(4 * WHICH) : "memory");
}
template <int WHICH> __device__ __forceinline__ void lds_bump() {
  asm volatile("s_mov_b64 exec, 1\n\tds_add_u32 %0, %1 offset:%2\n\ts_mov_b64 exec, -1" :: "v"((unsigned)L_CNT), "v"(1u), "n"(4 * WHICH) : "memory");
}
template <int WHICH, unsigned N> __device__ __forceinline__ void lds_wait() {
  MEMBAR();
  while ((unsigned)__builtin_amdgcn_readfirstlane(*(volatile LAS unsigned*)(L_CNT + 4 * WHICH)) < N) __builtin_amdgcn_s_sleep(1);
  MEMBAR();
}
template <int WHICH, int N> __device__ __forceinline__ void lds_spin() {
  unsigned t;
  asm volatile("1:\n\tv_mov_b32 %0, %1\n\tds_read_b32 %0, %0\n\ts_waitcnt lgkmcnt(0)\n\tv_cmp_gt_u32 vcc, %2, %0\n\ts_cbranch_vccnz 1b"
               : "=&v"(t) : "n"(L_CNT + 4 * WHICH), "n"(N) : "vcc", "memory");
}
__device__ __forceinline__ unsigned now() { return (unsigned)__builtin_amdgcn_s_memrealtime(); }
__device__ __forceinline__ void until(unsigned t) {
  while ((int)(now() - t) < 0) __builtin_amdgcn_s_sleep(2);
  MEMBAR();
}

__device__ __forceinline__ i32x4 mkrs(const void* p, int bytes) {
  const u64 a = (u64)p;
  return i32x4{(int)(unsigned)a, (int)((a >> 32) & 0xffff), bytes, 0x00020000};
}
template <int N> __device__ __forceinline__ void wait_vm() { asm volatile("s_waitcnt vmcnt(%0)" ::"n"(N) : "memory"); }
template <int W>
__device__ __forceinline__ void dma_tile(i32x4 rs, unsigned ldsb, int soff, const int (&v)[4]) {
  asm volatile("s_mov_b32 m0, %1\n\ts_waitcnt vmcnt(%7) lgkmcnt(0)\n\t"
               "buffer_load_dwordx4 %3, %0, %2 offen nt lds\n\ts_waitcnt vmcnt(%7)\n\t"
               "buffer_load_dwordx4 %4, %0, %2 offen offset:1024 nt lds\n\ts_waitcnt vmcnt(%7)\n\t"
               "buffer_load_dwordx4 %5, %0, %2 offen offset:2048 nt lds\n\ts_waitcnt vmcnt(%7)\n\t"
               "buffer_load_dwordx4 %6, %0, %2 offen offset:3072 nt lds"
               :: "s"(rs), "s"(ldsb), "s"(soff), "v"(v[0]), "v"(v[1]), "v"(v[2]), "v"(v[3]), "n"(W - 1) : "memory");
}
__device__ __forceinline__ void st_dev(__amdgpu_buffer_rsrc_t rs, int off, u32x4 d) { __builtin_amdgcn_raw_buffer_store_b128(d, rs, off, 0, SC1); }

template <int N>
__device__ __forceinline__ void await_hint(const char* p, unsigned tag) {
  _Pragma("nounroll") for (int spin = 0; spin < 64; ++spin) {
    unsigned bad = 0;
    if constexpr (N == 16) {
      u32x16 y;
      asm volatile("s_load_dwordx16 %0, %1, 0x0 glc\n\ts_waitcnt lgkmcnt(0)" : "=s"(y) : "s"(p) : "memory");
#pragma unroll
      for (int i = 0; i < 16; ++i) bad |= y[i] ^ tag;
    } else {
      u32x4 y;
      asm volatile("s_load_dwordx4 %0, %1, 0x0 glc\n\ts_waitcnt lgkmcnt(0)" : "=s"(y) : "s"(p) : "memory");
#pragma unroll
      for (int i = 0; i < 4; ++i) bad |= y[i] ^ tag;
    }
    if (!bad) break;
  }
}

__device__ __forceinline__ void wtrans(const u32x4 (&w)[4], u32x4 (&a)[4]) {
#pragma unroll
  for (int i = 0; i < 4; ++i)
#pragma unroll
    for (int p = 0; p < 2; ++p) {
      const unsigned e0 = __builtin_amdgcn_update_dpp(w[i][p], w[i][2 + p], 0x128, 0xf, 0xc, false);
      const unsigned e1 = __builtin_amdgcn_update_dpp(w[i][2 + p], w[i][p], 0x128, 0xf, 0x3, false);
      a[2 * p][i] = __builtin_amdgcn_perm(e1, e0, 0x05040100u);
      a[2 * p + 1][i] = __builtin_amdgcn_perm(e1, e0, 0x07060302u);
    }
}
template <class F>
__device__ __forceinline__ void proj_reduce(LAS char* const (&ra)[4], const f32x4 (&acc)[2][4], float (&r)[8], F&& meet) {
#pragma unroll
  for (int x = 1; x < 4; ++x)
#pragma unroll
    for (int jj = 0; jj < 4; ++jj)
      *(LAS f32x2*)(ra[x] + (x - 1) * 2048 + jj * 512) = f32x2{acc[x >> 1][jj][2 * (x & 1)], acc[x >> 1][jj][2 * (x & 1) + 1]};
  meet();
#pragma unroll
  for (int jj = 0; jj < 4; ++jj) {
    f32x2 s = {acc[0][jj][0], acc[0][jj][1]};
#pragma unroll
    for (int x = 1; x < 4; ++x) s += *(LAS f32x2*)(ra[0] + (x - 1) * 2048 + jj * 512);
    r[jj] = s[0]; r[4 + jj] = s[1];
  }
}

#define STR_(x) #x
#define R4(x) "v[" STR_(x) ":" STR_(x) "+3]"
#define R2(x) "v[" STR_(x) ":" STR_(x) "+1]"
#define R1(x) "v[" STR_(x) "]"
#define G_MF(s, j, C) "v_mfma_f32_16x16x32_bf16 " R4(192+4*j) ", " R4(16*s+4*j) ", " R4(160+4*s) ", " C "\n\t"
#define G_S4(s, F0, F1, F2, F3) G_MF(s, 0, R4(192)) F0 G_MF(s, 1, R4(196)) F1 G_MF(s, 2, R4(200)) F2 G_MF(s, 3, R4(204)) F3
#define G_MXB(s) "v_max3_u32 %[mx], %[mx], " R1(160+4*s+2) ", " R1(160+4*s+3) "\n\t"
#define G_MX(s) "v_max3_u32 %[mx], %[mx], " R1(160+4*s) ", " R1(160+4*s+1) "\n\t" G_MXB(s)
#define G_ADD(A, i) "v_pk_add_f32 " R2(A+2*i) ", " R2(A+2*i) ", " R2(192+2*i) "\n\t"
#define G_ADD8(A) G_ADD(A, 0) G_ADD(A, 1) G_ADD(A, 2) G_ADD(A, 3) G_ADD(A, 4) G_ADD(A, 5) G_ADD(A, 6) G_ADD(A, 7)
#define G_W(n) "s_waitcnt vmcnt(" #n ")\n\t"
#define G_LD(s, SO, OFF) "buffer_load_dwordx4 " R4(160+4*s) ", %[vo], %[rs], " SO " offen" OFF " sc1\n\t"
#define G_LD0 G_LD(0, "%[so0]", "")
#define G_LD1 G_LD(1, "%[so0]", " offset:1024")
#define G_LD2 G_LD(2, "%[so0]", " offset:2048")
#define G_LD3 G_LD(3, "%[so0]", " offset:3072")
#define G_LD4 G_LD(4, "%[so1]", "")
#define G_LD5 G_LD(5, "%[so1]", " offset:1024")
#define G_LD6 G_LD(6, "%[so1]", " offset:2048")
#define G_LD7 G_LD(7, "%[so1]", " offset:3072")
#define G_HINT "s_load_dwordx16 s[68:83], %[hp], 0x0 glc\n\t"
#define G_B(i) "s_cmp_eq_u32 s" #i ", %[tag]\n\ts_addc_u32 s85, s85, s85\n\t"
#define G_MASK G_B(83) G_B(82) G_B(81) G_B(80) G_B(79) G_B(78) G_B(77) G_B(76) G_B(75) G_B(74) G_B(73) G_B(72) G_B(71) G_B(70) G_B(69) G_B(68)
#define G_HINT_REGS "s68", "s69", "s70", "s71", "s72", "s73", "s74", "s75", "s76", "s77", "s78", "s79", "s80", "s81", "s82", "s83", "s85"
#define G_RDY "ds_read_b32 %[rdy], %[cnt] offset:%[crdy]\n\t"
#define G_SPIN "s_add_u32 s93, s93, 1\n\ts_cmp_gt_u32 s93, %[cap]\n\ts_cbranch_scc1 99f\n\t"
#define G_FIRST "v_ffbl_b32 %[av], %[vo]\n\tv_lshlrev_b32 %[vo], 15, %[av]\n\t"
#define G_OFFS "v_bitop3_b32 %[vo], %[vo], s92, %[oo] bitop3:0x6a\n\t"
#define G_NXT "v_lshlrev_b32 %[nxt], %[av], 1\n\t"
#define G_LOOP \
  G_HINT "v_mov_b32 %[pend], 15\n\ts_mov_b32 s93, 0\n\ts_mov_b32 s92, 0x40018000\n\t" \
  "s_barrier\n\ts_waitcnt lgkmcnt(0)\n\t" G_MASK \
  "v_bfe_u32 %[vo], s85, %[sh4], 4\n\t" \
  "v_cmp_ne_u32 vcc, 0, %[vo]\n\t" \
  G_FIRST G_NXT G_OFFS \
  "s_cbranch_vccz 84f\n\t" G_LD0 G_LD1 G_LD2 G_LD3 "\n" \
  "84:\n\ts_barrier\n\ts_setprio 3\n\ts_cbranch_vccnz 82f\n" \
  "90:\n\t" G_RDY G_SPIN \
  "s_waitcnt lgkmcnt(0)\n\t" \
  "v_or_b32 %[rdy], s85, %[rdy]\n\t" \
  "v_bfe_u32 %[vo], %[rdy], %[sh4], 4\n\t" \
  "v_and_b32 %[vo], %[vo], %[pend]\n\t" \
  "v_cmp_ne_u32 vcc, 0, %[vo]\n\t" \
  "s_cbranch_vccz 90b\n\t" \
  G_FIRST G_NXT G_OFFS G_LD0 G_LD1 G_LD2 G_LD3 "\n" \
  "82:\n\t" G_LD4 G_LD5 G_LD6 G_LD7 \
  "v_and_b32 %[cur], %[nxt], %[pend]\n\t" \
  G_W(7) G_RDY "\n" \
  "91:\n\t" G_W(7) \
  G_MF(0, 0, "0") "v_max_u32 %[mx], v160, v161\n\tv_bfi_b32 %[vo], %[cur], 0, %[pend]\n\t" \
  G_MF(0, 1, "0") G_MXB(0) "v_and_b32 %[av], %[t0], %[cur]\n\t" \
  G_MF(0, 2, "0") "v_cmp_ne_u32 s[88:89], 0, %[av]\n\tv_cmp_ne_u32 s[90:91], %[cur], %[av]\n\t" \
  G_MF(0, 3, "0") "s_waitcnt vmcnt(6) lgkmcnt(0)\n\tv_or_b32 %[rdy], s85, %[rdy]\n\t" \
  G_MF(1, 0, R4(192)) "v_bfe_u32 %[av], %[rdy], %[sh4], 4\n\tv_and_b32 %[vo], %[av], %[vo]\n\t" \
  G_MF(1, 1, R4(196)) "v_cmp_ne_u32 vcc, 0, %[vo]\n\t" G_FIRST \
  G_MF(1, 2, R4(200)) G_NXT "s_cbranch_vccz 96f\n\t" G_OFFS \
  G_MF(1, 3, R4(204)) G_LD0 G_W(6) \
  G_S4(2, G_MX(1), G_LD1, G_MX(2), G_LD2 G_W(7)) \
  G_S4(3, G_MX(3), "", "", G_LD3 G_W(7)) \
  G_S4(4, G_MX(4), "", "", G_LD4 G_W(7)) \
  G_S4(5, G_MX(5), "", "", G_LD5 G_W(7)) \
  G_S4(6, G_MX(6), "", "", G_LD6 G_W(7)) \
  G_S4(7, G_MX(7), "v_cmp_eq_u32 vcc, -1, %[mx]\n\t", G_RDY, G_LD7) \
  "s_branch 97f\n" \
  "95:\n\t" G_SPIN \
  "s_or_b32 s86, vcc_lo, vcc_hi\n\t" \
  "s_lshr_b32 s87, s86, 16\n\ts_or_b32 s86, s86, s87\n\ts_lshr_b32 s87, s86, 2\n\ts_or_b32 s86, s86, s87\n\ts_lshr_b32 s87, s86, 1\n\ts_or_b32 s86, s86, s87\n\t" \
  "s_and_b32 s86, s86, 0x1111\n\ts_mul_i32 s86, s86, 0xf000f\n\ts_mov_b32 s87, s86\n\t" \
  "s_andn2_b64 s[88:89], s[88:89], s[86:87]\n\ts_andn2_b64 s[90:91], s[90:91], s[86:87]\n\t" \
  "s_branch 92f\n" \
  "96:\n\t" \
  G_MF(1, 3, R4(204)) G_W(5) \
  G_S4(2, G_MX(1), "", G_MX(2), G_W(4)) \
  G_S4(3, G_MX(3), "", "", G_W(3)) \
  G_S4(4, G_MX(4), "", "", G_W(2)) \
  G_S4(5, G_MX(5), "", "", G_W(1)) \
  G_S4(6, G_MX(6), "", "", G_W(0)) \
  G_S4(7, G_MX(7), "v_cmp_eq_u32 vcc, -1, %[mx]\n\t", "s_nop 0\n\t", "\n") \
  "97:\n\t" \
  "s_cbranch_vccnz 95b\n" \
  "92:\n\t" \
  "s_mov_b64 exec, s[88:89]\n\t" \
  "s_cbranch_execz 93f\n\t" \
  "v_xor_b32 %[pend], %[pend], %[cur]\n\t" G_ADD8(128) "\n" \
  "93:\n\t" \
  "s_mov_b64 exec, s[90:91]\n\t" \
  "s_cbranch_execz 94f\n\t" \
  "v_xor_b32 %[pend], %[pend], %[cur]\n\t" G_ADD8(144) "\n" \
  "94:\n\t" \
  "s_mov_b64 exec, -1\n\t" \
  "v_and_b32 %[cur], %[nxt], %[pend]\n\t" \
  "v_cmp_ne_u32 vcc, 0, %[cur]\n\t" \
  "s_cbranch_vccnz 91b\n\t" \
  "v_cmp_ne_u32 vcc, 0, %[pend]\n\t" \
  "s_cbranch_vccnz 90b\n" \
  "99:\n\ts_waitcnt vmcnt(0) lgkmcnt(0)"

__global__ __launch_bounds__(512, 2) void gqa_fused(const char* __restrict__ x, const char* __restrict__ kc, const char* __restrict__ vc,
                                                    const char* __restrict__ wq, const char* __restrict__ wo, char* __restrict__ out, char* __restrict__ ws) {
  extern __shared__ __attribute__((aligned(16))) char smem[];
  LAS char* L = (LAS char*)smem;
  const int tid = threadIdx.x, lane = tid & 63, wave = __builtin_amdgcn_readfirstlane(tid >> 6);
  const int wg = blockIdx.x;
  {
    u64 pc; int code_len;
    asm volatile("s_getpc_b64 %0\n.Lgqa_pc:\n\ts_mov_b32 %1, .Lgqa_end-.Lgqa_pc" : "=s"(pc), "=s"(code_len));
    asm volatile("s_mov_b32 m0, %3\n\ts_nop 0\n\tbuffer_load_dword %0, %1, %2 offen lds"
                 :: "v"(lane * 128 + wave * 65536), "s"(mkrs((const void*)pc, code_len)), "s"((wg & 24) * 1024), "n"(L_SINK) : "memory");
    const int row = (wg >> 3) * 8 + wave;
    int qv = row * 32768 + (wg & 7) * 1024 + lane * 16, xv = row * 1024 + lane * 16;
    asm volatile("" : "+v"(qv), "+v"(xv));
    SB();
    asm volatile("s_mov_b32 m0, %4\n\ts_movk_i32 vcc_lo, 0x2000\n\t"
                 "global_load_lds_dwordx4 %0, %2\n\tv_add_u32 %0, vcc_lo, %0\n\tglobal_load_lds_dwordx4 %0, %2\n\tv_add_u32 %0, vcc_lo, %0\n\t"
                 "global_load_lds_dwordx4 %0, %2\n\tv_add_u32 %0, vcc_lo, %0\n\tglobal_load_lds_dwordx4 %0, %2\n\tglobal_load_lds_dwordx4 %1, %3"
                 : "+v"(qv) : "v"(xv), "s"(wq), "s"(x), "n"(L_SINK), "s"(kc), "s"(vc), "s"(wo), "s"(out), "s"(ws) : "vcc", "memory");
    SB();
  }
  unsigned tstart = now();
  *((LAS unsigned*)(L + L_CNT) + (lane & 15)) = 0u;
  asm volatile("s_waitcnt lgkmcnt(0)\n\ts_barrier" : "+s"(tstart) :: "memory");

  const int c = lane & 15, g = lane >> 4;
  const int b = wg >> 3, hkv = (wg >> 1) & 3, half = wg & 1, rg = wg >> 6, i8 = b & 7;
  const int grp = rg * 4 + hkv;
  const int oslot = WS_O + ((grp * 2 + (i8 >> 2)) * 2 + half) * 32768 + (i8 & 3) * 256;
  const char* hp = ws + WS_HINT + wg * 128;
  const __amdgpu_buffer_rsrc_t rws = __builtin_amdgcn_make_buffer_rsrc((void*)ws, 0, WS_BYTES, 0x00020000);

  if (wave >= 4) {
    const int v = wave - 4, tv = v >> 1, hf = v & 1;
    __builtin_amdgcn_s_setprio(3);
    const unsigned epoch = __builtin_amdgcn_raw_buffer_load_b32(rws, 0, WS_E, SC1);
#pragma unroll
    for (int e = 0; e < 2; ++e) st_dev(rws, oslot + v * 1024 + (e * 4 + g) * 4096 + c * 16, u32x4{~0u, ~0u, ~0u, ~0u});
    const int wcol = ((lane & 7) ^ (2 * hf)) * 16, wb = (lane >> 3) & 1;
    f32x4 acc[2][4];
    const int ct = (wg & 7) * 8 + i8;
    {
      const __amdgpu_buffer_rsrc_t rq = __builtin_amdgcn_make_buffer_rsrc((void*)(wq + (size_t)v * 256 * 8192 + ct * 128), 0, 256 * 8192 - ct * 128, 0x00020000);
      const __amdgpu_buffer_rsrc_t rx = __builtin_amdgcn_make_buffer_rsrc((void*)(x + rg * 65536), 0, 65536, 0x00020000);
      const int wv = (g * 8 + wb) * 8192 + wcol, xv0 = (tv * 16 + (lane >> 2)) * 2048 + v * 512 + (lane & 3) * 16, xv1 = xv0 ^ 32768;
      const int xsel = (4 * c + g) * 4;
      u32x4 wr[8][4], xr[8][2];
      until(tstart + QT0 + b * QT1);
#pragma unroll
      for (int s = 0; s < 8; ++s) {
        if (!(s & 1)) {
#pragma unroll
          for (int i = 0; i < 4; ++i) xr[s + (i & 1)][i >> 1] = __builtin_amdgcn_raw_buffer_load_b128(rx, ((i >> 1) ? xv1 : xv0) + (s + (i & 1)) * 64, 0, 0);
        }
#pragma unroll
        for (int i = 0; i < 4; ++i) wr[s][i] = __builtin_amdgcn_raw_buffer_load_b128(rq, wv, (s * 32 + 2 * i) * 8192, 0);
      }
      SB();
#pragma unroll
      for (int s = 0; s < 8; ++s) {
        u32x4 xf[2], a[4];
#pragma unroll
        for (int t = 0; t < 2; ++t)
#pragma unroll
          for (int u = 0; u < 4; ++u) xf[t][u] = __builtin_amdgcn_ds_bpermute(xsel, xr[s][t][u]);
        wtrans(wr[s], a);
#pragma unroll
        for (int jj = 0; jj < 4; ++jj)
#pragma unroll
          for (int t = 0; t < 2; ++t) acc[t][jj] = mfma16(a[jj], xf[t], s ? acc[t][jj] : f32x4{0.f, 0.f, 0.f, 0.f});
      }
    }
    const unsigned tag = __builtin_amdgcn_readfirstlane(epoch) + 1;
    LAS char* ra[4];
#pragma unroll
    for (int xw = 0; xw < 4; ++xw) { ra[xw] = L + L_SCR + (v ^ xw) * 6144 + lane * 8; asm volatile("" : "+v"(ra[xw])); }
    {
      float r[8];
      proj_reduce(ra, acc, r, [] { lds_signal<C_QRED>(); lds_spin<C_QRED, 4>(); });
      const auto s0 = __builtin_amdgcn_permlane32_swap(cvtpk(r[0], r[1]), cvtpk(r[4], r[5]), false, false);
      const auto s1 = __builtin_amdgcn_permlane32_swap(cvtpk(r[2], r[3]), cvtpk(r[6], r[7]), false, false);
      st_dev(rws, WS_Q + ((rg * 8 + tv * 4 + (c >> 2)) * 4 + hkv) * 8192 + (ct & 15) * 512 + hf * 256 + g * 64 + (c & 3) * 16, u32x4{s0[0], s1[0], s0[1], s1[1]});
      wait_vm<0>();
      const unsigned before = __builtin_amdgcn_readfirstlane(
          lane == 0 ? __hip_atomic_fetch_add((LAS unsigned*)(L + L_CNT) + C_QST, 1u, __ATOMIC_RELAXED, __HIP_MEMORY_SCOPE_WORKGROUP) : 0u);
      MEMBAR();
      if (before == 3) __builtin_amdgcn_raw_buffer_store_b32(tag, rws, lane < 3 ? WS_FLAG + (grp * 4 + lane) * 128 + (i8 * 2 + half) * 4 : FAR, 0, SC1);
    }
    const int cg = half * 8 + i8;
    u32x4 wr[8][4];
    {
      const __amdgpu_buffer_rsrc_t ro = __builtin_amdgcn_make_buffer_rsrc((void*)(wo + (size_t)(hkv * 1024 + v * 256) * 2048 + cg * 128), 0, 256 * 2048 - cg * 128, 0x00020000);
      const int wv = (g * 4 + wb) * 2048 + wcol;
#pragma unroll
      for (int s = 0; s < 8; ++s)
#pragma unroll
        for (int i = 0; i < 4; ++i) wr[s][i] = __builtin_amdgcn_raw_buffer_load_b128(ro, wv, ((s ^ (i8 & 4)) * 32 + (i >> 1) * 16 + (i & 1) * 2) * 2048, 0);
      SB();
    }
    {
      int qo = WS_Q + (wg >> 1) * 8192 + tv * 4096 + hf * 512 + (c >> 2) * 1024 + (g & 1) * 256 + (g >> 1) * 64 + (c & 3) * 16;
      asm volatile("" : "+v"(qo));
      if (v == 0) {
        await_hint<16>(ws + WS_FLAG + (grp * 4 + 1 + half) * 128, tag);
        _Pragma("nounroll") for (unsigned spin = 0; spin < SPIN_CAP; ++spin) {
          MEMBAR();
          if (__all(__builtin_amdgcn_raw_buffer_load_b32(rws, WS_FLAG + grp * 512 + c * 4, 0, SC1) == tag)) break;
        }
        *((LAS unsigned*)(L + L_CNT) + C_TAG) = tag;
        lds_signal<C_FLAG>();
      } else {
        lds_wait<C_FLAG, 1>();
      }
      u32x4 d[2];
#pragma unroll
      for (int e = 0; e < 2; ++e) d[e] = __builtin_amdgcn_raw_buffer_load_b128(rws, qo + e * 128, 0, SC1);
#pragma unroll
      for (int e = 0; e < 2; ++e) *(LAS u32x4*)(L + L_SCR + v * 6144 + e * 1024 + lane * 16) = d[e];
      lds_signal<C_QRDY>();
    }
    __builtin_amdgcn_s_setprio(0);
    u32x32 wf0, wf1, wf2, wf3;
    {
      auto pack = [&](int s2, u32x32& o) {
#pragma unroll
        for (int e = 0; e < 2; ++e) {
          u32x4 a[4];
          wtrans(wr[s2 + e], a);
#pragma unroll
          for (int i = 0; i < 16; ++i) o[e * 16 + i] = a[i >> 2][i & 3];
        }
      };
      pack(0, wf0); asm volatile("" : "+{v[0:31]}"(wf0)); SB();
      pack(2, wf1); asm volatile("" : "+{v[32:63]}"(wf1)); SB();
      pack(4, wf2); asm volatile("" : "+{v[64:95]}"(wf2)); SB();
      pack(6, wf3); asm volatile("" : "+{v[96:127]}"(wf3)); SB();
    }
    __builtin_amdgcn_s_setprio(3);
    if (v == 0) {
      lds_wait<C_ML, 4>();
      const int qi = lane & 31;
      f32x2 ml[4];
#pragma unroll
      for (int i = 0; i < 4; ++i) ml[i] = *(LAS f32x2*)(L + L_ML + (i * 32 + qi) * 8);
      const float M = fmaxf(fmaxf(ml[0][0], ml[1][0]), fmaxf(ml[2][0], ml[3][0]));
      float Ls = 0.f;
#pragma unroll
      for (int i = 0; i < 4; ++i) Ls += ml[i][1] * __builtin_amdgcn_exp2f(ml[i][0] - M);
      st_dev(rws, lane < 32 ? WS_ML + wg * 512 + qi * 16 : FAR, u32x4{__float_as_uint(M), tag, __float_as_uint(Ls), tag});
      lds_wait<C_MLRD, 1>();
      const char* mp = ws + WS_ML + (wg ^ 1) * 512;
      _Pragma("nounroll") for (int spin = 0; spin < 32; ++spin) {
        unsigned y0, y1;
        asm volatile("s_load_dword %0, %2, 0x4 glc\n\ts_load_dword %1, %2, 0x1fc glc\n\ts_waitcnt lgkmcnt(0)" : "=&s"(y0), "=&s"(y1) : "s"(mp) : "memory");
        if (y0 == tag && y1 == tag) break;
      }
      u32x4 d;
      _Pragma("nounroll") for (unsigned spin = 0; spin < SPIN_CAP; ++spin) {
        MEMBAR();
        d = __builtin_amdgcn_raw_buffer_load_b128(rws, WS_ML + (wg ^ 1) * 512 + qi * 16, 0, SC1);
        if (__all(d[1] == tag && d[3] == tag)) break;
        __builtin_amdgcn_s_sleep(8);
      }
      const float M2 = __uint_as_float(d[0]), L2 = __uint_as_float(d[2]);
      const float Mt = fmaxf(M, M2);
      const float inv = 1.f / (Ls * __builtin_amdgcn_exp2f(M - Mt) + L2 * __builtin_amdgcn_exp2f(M2 - Mt));
#pragma unroll
      for (int i = 0; i < 4; ++i) *(LAS float*)(L + L_F + (i * 32 + qi) * 4) = __builtin_amdgcn_exp2f(ml[i][0] - Mt) * inv;
      lds_signal<C_F>();
    } else if (v == 3 && wg == 0) {
      lds_wait<C_ML, 4>();
      _Pragma("nounroll") for (unsigned spin = 0; spin < SPIN_CAP; ++spin) {
        MEMBAR();
        const u32x4 fl = __builtin_amdgcn_raw_buffer_load_b128(rws, WS_FLAG + (lane >> 2) * 512 + (lane & 3) * 16, 0, SC1);
        if (__all(((fl[0] ^ tag) | (fl[1] ^ tag) | (fl[2] ^ tag) | (fl[3] ^ tag)) == 0)) break;
        __builtin_amdgcn_s_sleep(16);
      }
      __builtin_amdgcn_raw_buffer_store_b32(tag, rws, lane == 0 ? WS_E : FAR, 0, SC1);
    }
    __builtin_amdgcn_s_setprio(0);
    int pst = WS_P + wg * 8192 + tv * 4096 + hf * 1024 + (c >> 3) * 2048 + g * 128 + (c & 7) * 16;
    int pld = WS_P + (wg & ~6) * 8192 + hkv * 2048 + v * 512 + lane * 8;
    int pho = (v == 0 && lane < 4) ? WS_PH + ((wg & ~6) + lane * 2) * 128 + hkv * 4 : FAR;
    int ost = (rg * 32 + hkv * 8 + (c >> 1)) * 2048 + cg * 128 + (g & 1) * 64 + v * 16 + (g >> 1) * 8 + (c & 1) * 4;
    const unsigned spread = tag * 0x01010101u;
    u32x4 tb = {spread, spread >> 8, spread >> 16, spread >> 24};
    u32x2 tl = {(spread >> (16 * (lane & 1))) & 0xFFu, (spread >> (16 * (lane & 1) + 8)) & 0xFFu};
    {
      const int rot = i8 & 3;
      int oo = (c >> 2) * 256 + g * 64 + (c & 3) * 16 + rot * 32768, sh4 = c & 12;
      unsigned cnt = L_CNT;
      i32x4 rs = mkrs(ws, WS_BYTES);
      int so0 = WS_O + grp * 131072 + v * 8192 + (i8 & 4) * 1024, so1 = so0 ^ 4096, t0 = ((rot >> 1) ^ tv) ? 12 : 3;
      f32x32 res = 0.f;
      asm volatile("" : "+v"(oo), "+v"(sh4), "+v"(cnt), "+v"(ra[0]), "+v"(ra[1]), "+v"(ra[2]), "+v"(ra[3]), "+v"(pst), "+v"(pld), "+v"(ost), "+v"(pho), "+v"(tb), "+v"(tl),
                        "+{v[128:159]}"(res), "+s"(rs), "+s"(so0), "+s"(so1), "+s"(t0), "+s"(hp));
      lds_wait<C_MLRD, 4>();
      lds_spin<C_LAST, 4>();
      u32x32 buf;
      unsigned vo, mx, av, pend, cur, nxt, rdy;
      asm volatile(
          G_LOOP
          : "+{v[128:159]}"(res), "=&{v[160:191]}"(buf), [vo] "=&v"(vo), [mx] "=&v"(mx), [av] "=&v"(av), [pend] "=&v"(pend), [cur] "=&v"(cur),
            [nxt] "=&v"(nxt), [rdy] "=&v"(rdy)
          : "{v[0:31]}"(wf0), "{v[32:63]}"(wf1), "{v[64:95]}"(wf2), "{v[96:127]}"(wf3), [oo] "v"(oo), [sh4] "v"(sh4), [cnt] "v"(cnt), [hp] "s"(hp),
            [rs] "s"(rs), [so0] "s"(so0), [so1] "s"(so1), [t0] "s"(t0), [tag] "s"(tag), [cap] "n"(SPIN_CAP * 16), [crdy] "n"(4 * C_RDY)
          : "memory", "vcc", "scc", G_HINT_REGS, "s86", "s87", "s88", "s89", "s90", "s91", "s92", "s93", "v192", "v193", "v194", "v195", "v196", "v197",
            "v198", "v199", "v200", "v201", "v202", "v203", "v204", "v205", "v206", "v207");
#pragma unroll
      for (int i = 0; i < 32; ++i) acc[i >> 4][(i >> 2) & 3][i & 3] = res[i];
    }
    {
      float r[8];
      proj_reduce(ra, acc, r, [&] {
        asm volatile("s_waitcnt lgkmcnt(0)\n\ts_barrier" ::: "memory");
        __builtin_amdgcn_raw_buffer_store_b32(tag, rws, pho, 0, SC1);
      });
#pragma unroll
      for (int e = 0; e < 2; ++e)
        st_dev(rws, pst + e * 512, u32x4{tag8(r[4 * e], tb[0]), tag8(r[4 * e + 1], tb[1]), tag8(r[4 * e + 2], tb[2]), tag8(r[4 * e + 3], tb[3])});
    }
    await_hint<4>(ws + WS_PH + wg * 128, tag);
    {
      u32x2 d[4];
      _Pragma("nounroll") for (unsigned spin = 0; spin < SPIN_CAP; ++spin) {
        MEMBAR();
#pragma unroll
        for (int i = 0; i < 4; ++i) d[i] = __builtin_amdgcn_raw_buffer_load_b64(rws, pld, i * 16384, SC1);
        unsigned mx = 0;
#pragma unroll
        for (int i = 0; i < 4; ++i) { d[i] ^= tl; mx |= d[i][0] | d[i][1]; }
        if (__all((mx & 0xFFu) == 0)) break;
      }
      const f32x2 s = (__builtin_bit_cast(f32x2, d[0]) + __builtin_bit_cast(f32x2, d[1])) + (__builtin_bit_cast(f32x2, d[2]) + __builtin_bit_cast(f32x2, d[3]));
      __builtin_amdgcn_raw_buffer_store_b32(cvtpk(s[0], s[1]), __builtin_amdgcn_make_buffer_rsrc((void*)out, 0, 262144, 0x00020000), ost, 0, 0);
    }
    asm volatile(".Lgqa_end:");
    __builtin_amdgcn_endpgm();
  } else {
    const int w = wave;
    const i32x4 rk = mkrs(kc + (size_t)wg * 262144, 262144), rv = mkrs(vc + (size_t)wg * 262144, 262144);
    const unsigned ring = w * 32768;
    int kv[4], vv[4];
#pragma unroll
    for (int k = 0; k < 4; ++k) {
      const int o = k * 3072 + w * 1024 + g * 256;
      kv[k] = o + (c ^ (4 * k + g)) * 16;
      vv[k] = o + (c ^ (2 * (4 * (k & 1) + g))) * 16;
    }
    auto issue = [&](auto jc) {
      constexpr int j = decltype(jc)::value;
      if constexpr (j < 16) dma_tile<WIN1>(rk, ring + (j & 7) * 4096, j * 16384, kv);
      else dma_tile<(j < 20 ? WIN1 : WIN2)>(rv, ring + (j & 7) * 4096, (j - 16) * 16384, vv);
    };
    LAS char* ka[4];
#pragma unroll
    for (int s = 0; s < 4; ++s) ka[s] = L + ring + c * 256 + (((2 * g) ^ c) ^ (8 * (s >> 1) + (s & 1))) * 16;
    {
      const i32x4 ro = mkrs(wo, 8 << 20);
      const int first = 9 * i8 - (i8 > 3 ? 8 : 0), last = first + (i8 == 3 ? 1 : 9);
      _Pragma("nounroll") for (int i = first; i < last; ++i)
        asm volatile("s_mov_b32 m0, %3\n\ts_waitcnt vmcnt(%4)\n\tbuffer_load_dwordx4 %0, %1, %2 offen lds"
                     :: "v"(lane * 16), "s"(ro), "s"((hkv * 1024 + (rg * 4 + w) * 64 + i) * 2048 + half * 1024), "n"(L_SINK), "n"(WINP - 1) : "memory");
    }
    u32x4 kr[12][4];
    sfor<0, 20>([&](auto jc) {
      constexpr int j = decltype(jc)::value, t = j - WIN1 / 4;
      if constexpr (j >= 14) lds_spin<C_STEP, 4 * (j - 12) - 1>();
      issue(jc);
      if constexpr (j >= 12 && j < 19) lds_bump<C_STEP>();
      if constexpr (t >= 0 && t < 12) {
#pragma unroll
        for (int s = 0; s < 4; ++s) kr[t][s] = *(LAS u32x4*)(ka[s] + (t & 7) * 4096);
      }
    });
    lds_wait<C_QRDY, 4>();
    __builtin_amdgcn_s_setprio(1);
    u32x4 q[2][4];
#pragma unroll
    for (int f = 0; f < 8; ++f) q[f >> 2][f & 3] = *(LAS u32x4*)(L + L_SCR + (f >> 1) * 6144 + (f & 1) * 1024 + lane * 16);
    f32x4 sc[16][2];
    float m[2] = {-__builtin_inff(), -__builtin_inff()};
    auto qk = [&](const u32x4 (&kf)[4], f32x4 (&o)[2]) {
#pragma unroll
      for (int qt = 0; qt < 2; ++qt) {
        f32x4 a = {0.f, 0.f, 0.f, 0.f};
#pragma unroll
        for (int s = 0; s < 4; ++s) a = mfma16(kf[s], q[qt][s], a);
        o[qt] = a;
      }
    };
    auto vmax = [&](const f32x4 (&t)[2]) {
#pragma unroll
      for (int qt = 0; qt < 2; ++qt) m[qt] = fmaxf(fmaxf(fmaxf(fmaxf(m[qt], t[qt][0]), t[qt][1]), t[qt][2]), t[qt][3]);
    };
    qk(kr[0], sc[0]);
    qk(kr[1], sc[1]);
    SB();
    sfor<0, 4>([&](auto nc) {
      constexpr int n = decltype(nc)::value;
      wait_vm<28>();
      u32x4 kf[4];
#pragma unroll
      for (int s = 0; s < 4; ++s) kf[s] = *(LAS u32x4*)(ka[s] + ((12 + n) & 7) * 4096);
      qk(kr[2 + n], sc[2 + n]);
      vmax(sc[n ? 11 + n : 0]);
      issue(ic<20 + n>{});
      qk(kf, sc[12 + n]);
      vmax(sc[1 + n]);
      SB();
    });
    sfor<6, 12>([&](auto tc) {
      constexpr int t = decltype(tc)::value;
      qk(kr[t], sc[t]);
      vmax(sc[t - 1]);
      if constexpr (t == 6) vmax(sc[15]);
      SB();
    });
    vmax(sc[11]);
#pragma unroll
    for (int qt = 0; qt < 2; ++qt) m[qt] = xg_max(m[qt]) * C2;
    u32x4 p[8][2];
    float l[2] = {0.f, 0.f};
    auto probs = [&](auto ec) {
      constexpr int e = decltype(ec)::value;
#pragma unroll
      for (int qt = 0; qt < 2; ++qt) {
        float ex[8];
#pragma unroll
        for (int j = 0; j < 8; ++j) { ex[j] = __builtin_amdgcn_exp2f(fmaf(sc[2 * e + (j >> 2)][qt][j & 3], C2, -m[qt])); l[qt] += ex[j]; }
        p[e][qt] = u32x4{cvtpk(ex[0], ex[1]), cvtpk(ex[2], ex[3]), cvtpk(ex[4], ex[5]), cvtpk(ex[6], ex[7])};
        asm volatile("" : "+v"(p[e][qt]));
      }
    };
    LAS char* va[8];
#pragma unroll
    for (int a = 0; a < 8; ++a) va[a] = L + ring + (lane >> 2) * 256 + ((a ^ (2 * w)) ^ ((lane >> 2) & 7)) * 32 + (lane & 3) * 8;
    f32x4 o[8][2];
    float f[4][2];
    int ooff[2];
    ooff[0] = oslot + w * 1024 + (c >> 2) * 4096 + g * 64 + (c & 3) * 16;
    ooff[1] = ooff[0] + 16384;
    int ho = lane < 16 ? WS_HINT + ((wg & 0xC6) | ((c >> 1) << 3) | (c & 1)) * 128 + ((i8 & 3) * 4 + ((((i8 >> 2) * 2) | half) ^ ((c >> 1) & 3))) * 4 : FAR;
    unsigned tg;
    LAS char* rd[4];
#pragma unroll
    for (int xw = 0; xw < 4; ++xw) rd[xw] = L + (w ^ xw) * 32768 + lane * 16;
    sfor<0, 8>([&](auto bc) {
      constexpr int bl = decltype(bc)::value, issued = bl < 4 ? 24 + 2 * bl : 32;
      if constexpr (bl < 2) sfor<5 * bl, 5 + 3 * bl>(probs);
      if constexpr (bl == 1) {
#pragma unroll
        for (int qt = 0; qt < 2; ++qt) {
          const float ls = xg_sum(l[qt]);
          if (g == 0) *(LAS f32x2*)(L + L_ML + (w * 32 + qt * 16 + c) * 8) = f32x2{m[qt], ls};
        }
        lds_signal<C_ML>();
      }
      SB();
      if constexpr (bl == 7) {
        lds_wait<C_F, 1>();
#pragma unroll
        for (int i = 0; i < 8; ++i) f[i >> 1][i & 1] = *(LAS float*)(L + L_F + c * 4 + ((w ^ (i >> 1)) * 32 + (i & 1) * 16) * 4);
        tg = *((LAS unsigned*)(L + L_CNT) + C_TAG);
        asm volatile("" : "+v"(f[0][0]), "+v"(f[0][1]), "+v"(f[1][0]), "+v"(f[1][1]), "+v"(f[2][0]), "+v"(f[2][1]), "+v"(f[3][0]), "+v"(f[3][1]),
                          "+v"(ooff[0]), "+v"(ooff[1]), "+v"(rd[0]), "+v"(rd[1]), "+v"(rd[2]), "+v"(rd[3]), "+v"(tg), "+v"(ho));
      }
      s16x4 vf[2][8];
      sfor<0, 2>([&](auto tc) {
        constexpr int t = decltype(tc)::value, j = 16 + 2 * bl + t;
        wait_vm<4 * (issued - 1 - j)>();
#pragma unroll
        for (int a = 7; a >= 0; --a) vf[t][a] = __builtin_amdgcn_ds_read_tr16_b64_v4i16((LAS s16x4*)(va[a] + (j & 7) * 4096));
      });
      if constexpr (bl == 7) { lds_bump<C_LAST>(); SB(); }
#pragma unroll
      for (int a = 7; a >= 0; --a) {
        const u32x2 lo = __builtin_bit_cast(u32x2, vf[0][a]), hi = __builtin_bit_cast(u32x2, vf[1][a]);
        const u32x4 af = {lo[0], lo[1], hi[0], hi[1]};
#pragma unroll
        for (int qt = 0; qt < 2; ++qt) {
          o[a][qt] = mfma16(af, p[bl][qt], bl ? o[a][qt] : f32x4{0.f, 0.f, 0.f, 0.f});
          if constexpr (bl == 7) { if (a < 6) *(LAS f32x4*)(rd[0] + ((a + 2) * 2 + qt) * 1024) = o[a + 2][qt]; SB(); }
        }
      }
      SB();
      if constexpr (bl < 4) {
        issue(ic<issued>{});
        if constexpr (bl == 2) lds_bump<C_MLRD>();
        issue(ic<issued + 1>{});
        if constexpr (bl == 3) __builtin_amdgcn_s_setprio(3);
        SB();
      }
    });
    f32x4 sum[2][2];
#pragma unroll
    for (int i = 0; i < 4; ++i) { sum[i >> 1][i & 1] = o[i & 1][i >> 1] * f[0][i >> 1]; asm volatile("" : "+v"(sum[i >> 1][i & 1])); }
    asm volatile("s_waitcnt lgkmcnt(0)\n\ts_barrier" ::: "memory");
    if (w == 0) __builtin_amdgcn_raw_buffer_store_b32(tg, rws, ho, 0, SC1);
    f32x4 part[2][3][2];
#pragma unroll
    for (int qt = 0; qt < 2; ++qt) {
#pragma unroll
      for (int xw = 1; xw < 4; ++xw)
#pragma unroll
        for (int e = 0; e < 2; ++e) part[qt][xw - 1][e] = *(LAS f32x4*)(rd[xw] + (xw * 4 + 2 * e + qt) * 1024);
      SB();
    }
#pragma unroll
    for (int qt = 0; qt < 2; ++qt) {
#pragma unroll
      for (int xw = 1; xw < 4; ++xw)
#pragma unroll
        for (int e = 0; e < 2; ++e) sum[qt][e] = __builtin_elementwise_fma(part[qt][xw - 1][e], f32x4{f[xw][qt], f[xw][qt], f[xw][qt], f[xw][qt]}, sum[qt][e]);
      const f32x4 s0 = sum[qt][0], s1 = sum[qt][1];
      st_dev(rws, ooff[qt], u32x4{nomark(cvtpk(s0[0], s0[1])), nomark(cvtpk(s0[2], s0[3])), nomark(cvtpk(s1[0], s1[1])), nomark(cvtpk(s1[2], s1[3]))});
      SB();
    }
    {
      const unsigned tag = __builtin_amdgcn_readfirstlane(tg);
      unsigned t;
      asm volatile("s_barrier\n\t"
                   "s_cmp_lt_u32 %[w], 3\n\ts_cbranch_scc1 73f\n\t"
                   "s_setprio 0\n\ts_mov_b32 s84, 64\n"
                   "70:\n\t" G_HINT "s_waitcnt lgkmcnt(0)\n\t" G_MASK
                   "s_sub_u32 s84, s84, 1\n\ts_cselect_b32 s85, -1, s85\n\t"
                   "v_mov_b32 %[t], s85\n\tds_write_b32 %[cnt], %[t] offset:%[crdy]\n\t"
                   "s_andn2_b32 s85, 0xffff, s85\n\ts_cbranch_scc1 70b\n"
                   "73:\n\ts_waitcnt lgkmcnt(0)"
                   : [t] "=&v"(t) : [hp] "s"(hp), [tag] "s"(tag), [w] "s"(w), [cnt] "v"((unsigned)L_CNT), [crdy] "n"(4 * C_RDY)
                   : "memory", "scc", G_HINT_REGS, "s84");
    }
    __builtin_amdgcn_endpgm();
  }
}

extern "C" void kernel_launch(void* const* d_in, const int* in_sizes, int n_in, void* d_out, int out_size, void* d_ws, size_t ws_size,
                              hipStream_t stream) {
  (void)hipFuncSetAttribute((const void*)gqa_fused, hipFuncAttributeMaxDynamicSharedMemorySize, LDS_BYTES);
  gqa_fused<<<256, 512, LDS_BYTES, stream>>>((const char*)d_in[0], (const char*)d_in[1], (const char*)d_in[2], (const char*)d_in[3],
                                             (const char*)d_in[4], (char*)d_out, (char*)d_ws);
}
```
